```python
import torch

def _softmax(x, axis=-1):
    return torch.softmax(x, dim=axis)
def _gelu(x):
    return torch.nn.functional.gelu(x, approximate="tanh")
def _silu(x):
    return torch.nn.functional.silu(x)
def _sigmoid(x):
    return torch.sigmoid(x)
def _layernorm(x, axis=-1, eps=1e-5):
    m = x.mean(dim=axis, keepdim=True)
    v = x.var(dim=axis, keepdim=True, unbiased=False)
    return (x - m) / torch.sqrt(v + eps)
def _scatter_add(tgt, idx, src, axis=0):
    return tgt.clone().index_add_(axis, idx, src)
def _segment_sum(src, seg, n):
    out = torch.zeros((n,) + src.shape[1:], dtype=src.dtype)
    return out.index_add_(0, seg, src)
def _pad(x, pw):
    flat = tuple(v for pair in reversed(pw) for v in pair)
    return torch.nn.functional.pad(x, flat)

kernel_name = 'gated_mix__sliding_local_dilated__mixer_s_mlp__prompt'

def setup_inputs():

    _w = (lambda a: (lambda t: t.to(torch.bfloat16) if t.is_floating_point() else t)(torch.as_tensor(a)))
    def _A_setup():
        B, S, D = 1, 2048, 640
        return {
            "x": _w((lambda *s: torch.randn(*s, dtype=torch.bfloat16) * 0.05)(B, S, D)),
            "wqkv": _w((lambda *s: torch.randn(*s, dtype=torch.bfloat16) * 3.2)(D, 3 * D)),
            "wo": _w((lambda *s: torch.randn(*s, dtype=torch.bfloat16) * 0.1)(D, D)),
        }
    _A_in = _A_setup()

    def _B_setup():
        B, S, D, F = 1, 2048, 640, 1728
        return {
            "x":  _w((lambda *s: torch.randn(*s, dtype=torch.bfloat16) * 0.05)(B, S, D)),
            "w1": _w((lambda *s: torch.randn(*s, dtype=torch.bfloat16) * 3.2)(D, D) * (D ** -0.5)),
            "w2": _w((lambda *s: torch.randn(*s, dtype=torch.bfloat16) * 3.2)(D, D) * (D ** -0.5)),
            "w3": _w((lambda *s: torch.randn(*s, dtype=torch.bfloat16) * 3.2)(D, F) * (D ** -0.5)),
            "w4": _w((lambda *s: torch.randn(*s, dtype=torch.bfloat16) * 3.2)(F, D) * (F ** -0.5)),
        }
    _B_in = _B_setup()

    return {
        "A_x": _A_in["x"],
        "A_wqkv": _A_in["wqkv"],
        "A_wo": _A_in["wo"],
        "B_x": _B_in["x"],
        "B_w1": _B_in["w1"],
        "B_w2": _B_in["w2"],
        "B_w3": _B_in["w3"],
        "B_w4": _B_in["w4"],
        "gate_w": _w((lambda *s: torch.randn(*s, dtype=torch.bfloat16) * 0.05)(640) * 16.0),
    }

def reference(A_x, A_wqkv, A_wo, B_x, B_w1, B_w2, B_w3, B_w4, gate_w):
    def _A_ref(x, wqkv, wo):
        B, S, D = x.shape
        H = 32; Dh = D // H
        W, dil = 8, 2
        qkv = (lambda x, *d: x.permute(*d))((x @ wqkv).reshape(B, S, 3, H, Dh), 2, 0, 3, 1, 4)
        q, k, v = qkv[0], qkv[1], qkv[2]
        scale = Dh ** -0.5
        pos = torch.arange(S)
        off = torch.arange(W) * dil
        idx = pos[:, None] - off[None, :]
        mask = idx >= 0
        idx_c = torch.where(mask, idx, 0).reshape(-1)
        kg = k[:, :, idx_c, :].reshape(B, H, S, W, Dh)
        vg = v[:, :, idx_c, :].reshape(B, H, S, W, Dh)
        logits = torch.einsum("bhsd,bhswd->bhsw", q, kg) * scale
        logits = torch.where(mask[None, None, :, :], logits, -1e9)
        attn = _softmax(logits, axis=-1)
        o = torch.einsum("bhsw,bhswd->bhsd", attn, vg)
        return (lambda x, *d: x.permute(*d))(o, 0, 2, 1, 3).reshape(B, S, D) @ wo

    def _B_ref(x, w1, w2, w3, w4):
        B, S, D = x.shape
        h = _gelu(_layernorm(x, axis=-1) @ w1)
        c = D // 4
        g0 = _pad(h[:, :, 0*c:1*c], ((0, 0), (1, 0), (0, 0)))[:, :S, :]
        g1 = _pad(h[:, :, 1*c:2*c], ((0, 0), (0, 1), (0, 0)))[:, 1:, :]
        g2 = _pad(h[:, :, 2*c:3*c], ((0, 0), (2, 0), (0, 0)))[:, :S, :]
        g3 = _pad(h[:, :, 3*c:   ], ((0, 0), (0, 2), (0, 0)))[:, 2:, :]
        hs = torch.concatenate([g0, g1, g2, g3], axis=-1) @ w2
        x1 = x + hs
        h2 = _gelu(_layernorm(x1, axis=-1) @ w3) @ w4
        return x1 + h2

    x = A_x
    g = _sigmoid((x * gate_w).sum(-1, keepdims=True))
    a = _A_ref(x, A_wqkv, A_wo)
    b = _B_ref(x, B_w1, B_w2, B_w3, B_w4)
    return g * a + (1 - g) * b
```

```cpp
#include <hip/hip_runtime.h>
#include <hip/hip_bf16.h>
typedef unsigned short u16;
typedef short bf16x8 __attribute__((ext_vector_type(8)));
typedef float f32x4 __attribute__((ext_vector_type(4)));

__device__ __forceinline__ float bf2f(u16 v){ return __uint_as_float(((unsigned)v)<<16); }
__device__ __forceinline__ u16 f2bf(float f){ __hip_bfloat16 b = __float2bfloat16(f); return *(u16*)&b; }
__device__ __forceinline__ float gelu_t(float v){
  float u = 0.7978845608028654f*(v + 0.044715f*v*v*v);
  float t = tanhf(u);
  return 0.5f*v*(1.f+t);
}
constexpr int S=2048, D=640, F=1728;

__global__ void k_ln(const u16* x, u16* xn, const u16* gw, float* g){
  int row = blockIdx.x; int t = threadIdx.x;
  float v[10]; float s=0.f, gs=0.f;
  for(int i=0;i<10;i++){ v[i]=bf2f(x[row*D+t+64*i]); s+=v[i]; if(gw) gs += v[i]*bf2f(gw[t+64*i]); }
  for(int o=32;o;o>>=1){ s+=__shfl_xor(s,o); gs+=__shfl_xor(gs,o);}
  float m=s/D; float q=0.f;
  for(int i=0;i<10;i++){ float d=v[i]-m; q+=d*d; }
  for(int o=32;o;o>>=1) q+=__shfl_xor(q,o);
  float r = rsqrtf(q/D+1e-5f);
  for(int i=0;i<10;i++) xn[row*D+t+64*i]=f2bf((v[i]-m)*r);
  if(gw && t==0) g[row]=1.f/(1.f+__expf(-gs));
}

template<int MODE>
__global__ __launch_bounds__(256) void k_gemm(const u16* A, const u16* W, int N, int K, u16* out, float* outf,
   const u16* res, const float* aa, const float* g){
  __shared__ u16 As[64][40];
  __shared__ u16 Bs[64][40];
  int bm = blockIdx.y*64, bn = blockIdx.x*64;
  int t=threadIdx.x, w=t>>6, l=t&63, wr=w>>1, wc=w&1, fr=l&15, fq=l>>4;
  f32x4 acc[2][2]={};
  for(int k0=0;k0<K;k0+=32){
    for(int i=t;i<64*32;i+=256){ int r=i>>5,c=i&31; int row=bm+r; int kk=k0+c; u16 val;
      if(MODE==2){ int grp=kk/160; int sh = grp==0?-1: grp==1?1: grp==2?-2:2; int rr=row+sh; val = (rr>=0&&rr<S)? A[rr*K+kk]:0; }
      else val = A[row*K+kk];
      As[r][c]=val; }
    for(int i=t;i<64*32;i+=256){ int kk=i>>6,n=i&63; Bs[n][kk]= W[(k0+kk)*N+bn+n]; }
    __syncthreads();
    bf16x8 a[2],b[2];
    for(int m=0;m<2;m++) a[m]=*(bf16x8*)&As[wr*32+m*16+fr][fq*8];
    for(int n=0;n<2;n++) b[n]=*(bf16x8*)&Bs[wc*32+n*16+fr][fq*8];
    for(int m=0;m<2;m++)for(int n=0;n<2;n++) acc[m][n]=__builtin_amdgcn_mfma_f32_16x16x32_bf16(a[m],b[n],acc[m][n],0,0,0);
    __syncthreads();
  }
  for(int m=0;m<2;m++)for(int n=0;n<2;n++)for(int j=0;j<4;j++){
    int row=bm+wr*32+m*16+fq*4+j, col=bn+wc*32+n*16+fr; float v=acc[m][n][j];
    if(MODE==0) out[row*N+col]=f2bf(v);
    if(MODE==1) out[row*N+col]=f2bf(gelu_t(v));
    if(MODE==2) out[row*N+col]=f2bf(bf2f(res[row*N+col])+v);
    if(MODE==3) outf[row*N+col]=v;
    if(MODE==4){ float gg=g[row]; float b=bf2f(res[row*N+col])+v; out[row*N+col]=f2bf(gg*aa[row*N+col]+(1.f-gg)*b); }
  }
}

__global__ void k_attn(const u16* qkv, u16* o){
  int idx = blockIdx.x*blockDim.x+threadIdx.x;
  int h = idx&31, s = idx>>5;
  float q[20]; for(int d=0;d<20;d++) q[d]=bf2f(qkv[s*1920+h*20+d]);
  float lg[8]; float mx=-1e30f;
  for(int w=0;w<8;w++){ int p=s-2*w; float a=0.f; if(p>=0){ for(int d=0;d<20;d++) a+=q[d]*bf2f(qkv[p*1920+640+h*20+d]); a*=0.22360679774997896f; } else a=-1e9f; lg[w]=a; mx=fmaxf(mx,a);}
  float sum=0.f; for(int w=0;w<8;w++){ lg[w]=__expf(lg[w]-mx); sum+=lg[w]; }
  float inv=1.f/sum; float acc[20]={};
  for(int w=0;w<8;w++){ int p=s-2*w; if(p>=0){ float pw=lg[w]*inv; for(int d=0;d<20;d++) acc[d]+=pw*bf2f(qkv[p*1920+1280+h*20+d]); } }
  for(int d=0;d<20;d++) o[s*640+h*20+d]=f2bf(acc[d]);
}

extern "C" void kernel_launch(void* const* d_in, const int* in_sizes, int n_in,
                              void* d_out, int out_size, void* d_ws, size_t ws_size,
                              hipStream_t stream) {
  const u16* x=(const u16*)d_in[0]; const u16* wqkv=(const u16*)d_in[1]; const u16* wo=(const u16*)d_in[2];
  const u16* w1=(const u16*)d_in[4]; const u16* w2=(const u16*)d_in[5]; const u16* w3=(const u16*)d_in[6]; const u16* w4=(const u16*)d_in[7];
  const u16* gw=(const u16*)d_in[8];
  char* ws=(char*)d_ws;
  u16* xn=(u16*)ws; ws+=S*D*2;
  u16* qkv=(u16*)ws; ws+=S*1920*2;
  u16* h=(u16*)ws; ws+=S*D*2;
  u16* o=(u16*)ws; ws+=S*D*2;
  u16* x1=(u16*)ws; ws+=S*D*2;
  u16* x1n=(u16*)ws; ws+=S*D*2;
  u16* h2=(u16*)ws; ws+=S*F*2;
  float* aa=(float*)ws; ws+=S*D*4;
  float* g=(float*)ws; ws+=S*4;
  k_ln<<<S,64,0,stream>>>(x,xn,gw,g);
  k_gemm<0><<<dim3(1920/64,S/64),256,0,stream>>>(x,wqkv,1920,D,qkv,nullptr,nullptr,nullptr,nullptr);
  k_gemm<1><<<dim3(D/64,S/64),256,0,stream>>>(xn,w1,D,D,h,nullptr,nullptr,nullptr,nullptr);
  k_attn<<<S*32/256,256,0,stream>>>(qkv,o);
  k_gemm<3><<<dim3(D/64,S/64),256,0,stream>>>(o,wo,D,D,nullptr,aa,nullptr,nullptr,nullptr);
  k_gemm<2><<<dim3(D/64,S/64),256,0,stream>>>(h,w2,D,D,x1,nullptr,x,nullptr,nullptr);
  k_ln<<<S,64,0,stream>>>(x1,x1n,nullptr,nullptr);
  k_gemm<1><<<dim3(F/64,S/64),256,0,stream>>>(x1n,w3,F,D,h2,nullptr,nullptr,nullptr,nullptr);
  k_gemm<4><<<dim3(D/64,S/64),256,0,stream>>>(h2,w4,D,F,(u16*)d_out,nullptr,x1,aa,g);
}
```

```cpp
#include <hip/hip_runtime.h>
#include <hip/hip_bf16.h>

typedef unsigned short u16;
typedef unsigned long long u64;
typedef unsigned u32x4 __attribute__((ext_vector_type(4)));
typedef unsigned u32x2 __attribute__((ext_vector_type(2)));
typedef short s16x4 __attribute__((ext_vector_type(4)));
typedef short s16x8 __attribute__((ext_vector_type(8)));
typedef float f32x4 __attribute__((ext_vector_type(4)));
typedef float f32x2 __attribute__((ext_vector_type(2)));
typedef __bf16 bf16x2_t __attribute__((ext_vector_type(2)));
#define LAS __attribute__((address_space(3)))
#define GAS __attribute__((address_space(1)))
typedef __amdgpu_buffer_rsrc_t rsrc_t;

#define DBG_STAMPS 0

constexpr int S_ = 2048, D_ = 640, F_ = 1728, Q_ = 1920;
constexpr unsigned WS_EPOCH = 0;
constexpr unsigned WS_FH   = 4096;
constexpr unsigned WS_FQ   = WS_FH + 16384;
constexpr unsigned WS_FX   = WS_FQ + 16384;
constexpr unsigned WS_FO   = WS_FX + 16384;
constexpr unsigned WS_FG   = WS_FO + 16384;
constexpr unsigned WS_FC   = WS_FG + 16384;
constexpr unsigned WS_FLAGS_END = WS_FC + 16384;
constexpr unsigned WS_H    = 131072;
constexpr unsigned WS_QKV  = WS_H + S_ * D_ * 2;
constexpr unsigned WS_X1   = WS_QKV + S_ * Q_ * 2;
constexpr unsigned WS_O    = WS_X1 + S_ * D_ * 2;
constexpr unsigned WS_H2   = WS_O + S_ * D_ * 2;
constexpr unsigned WS_ST   = WS_H2 + S_ * F_ * 2;
constexpr unsigned ST_BYTES = 22 * 20 * 96 * 8;
constexpr unsigned WS_CS   = WS_ST + ST_BYTES;
constexpr unsigned WS_DBG  = WS_CS + 8192;
constexpr unsigned SLOT = 32768, BOFF = 16384;
constexpr unsigned L_ST = 131072, L_CS = L_ST + 20480, L_GW = L_CS + 1024, L_ONE = L_GW + 2048, L_EP = L_ONE + 64, L_SCR = L_EP + 64;
constexpr unsigned LDS_BYTES = L_SCR + 2048;
constexpr u64 EPOCH_STEP = 0x9E3779B97F4A7C15ull;
constexpr unsigned BIGOFF = 0x7fffff00u;
constexpr unsigned SPIN_CAP = 1u << 17;

template <int N> __device__ __forceinline__ void waitv() { asm volatile("s_waitcnt vmcnt(%0)" ::"i"(N) : "memory"); }
__device__ __forceinline__ void bar() { asm volatile("" ::: "memory"); __builtin_amdgcn_s_barrier(); asm volatile("" ::: "memory"); }
__device__ __forceinline__ unsigned cvt_pk(float lo, float hi) { f32x2 f = {lo, hi}; bf16x2_t b = __builtin_convertvector(f, bf16x2_t); return __builtin_bit_cast(unsigned, b); }
__device__ __forceinline__ float bflo(unsigned v) { return __uint_as_float(v << 16); }
__device__ __forceinline__ float bfhi(unsigned v) { return __uint_as_float(v & 0xffff0000u); }
__device__ __forceinline__ float dot2(unsigned a, unsigned b, float c) { return __builtin_amdgcn_fdot2_f32_bf16(__builtin_bit_cast(bf16x2_t, a), __builtin_bit_cast(bf16x2_t, b), c, false); }
__device__ __forceinline__ rsrc_t mkr(const void* p, unsigned bytes) { return __builtin_amdgcn_make_buffer_rsrc((void*)p, (short)0, (int)bytes, 0x00020000); }
__device__ __forceinline__ void dma(unsigned m0v, unsigned vo, rsrc_t r, unsigned so) {
  asm volatile("s_mov_b32 m0, %0\n\ts_nop 0\n\tbuffer_load_dwordx4 %1, %2, %3 offen lds" ::"s"(m0v), "v"(vo), "s"(r), "s"(so) : "memory");
}
__device__ __forceinline__ void dma_sc1(unsigned m0v, unsigned vo, rsrc_t r, unsigned so) {
  asm volatile("s_mov_b32 m0, %0\n\ts_nop 0\n\tbuffer_load_dwordx4 %1, %2, %3 offen sc1 lds" ::"s"(m0v), "v"(vo), "s"(r), "s"(so) : "memory");
}
__device__ __forceinline__ float gelu_t(float x) {
  const float k1 = -2.0f * 0.7978845608028654f * 1.4426950408889634f, k2 = k1 * 0.044715f;
  float t = x * __builtin_fmaf(x * x, k2, k1);
  float e = __builtin_amdgcn_exp2f(t);
  return x * __builtin_amdgcn_rcpf(1.0f + e);
}
__device__ __forceinline__ void waitv_dyn(int n) {
  if (n >= 16) waitv<16>(); else if (n >= 13) waitv<13>(); else if (n >= 10) waitv<10>(); else if (n >= 8) waitv<8>();
  else if (n >= 7) waitv<7>(); else if (n >= 5) waitv<5>(); else if (n >= 4) waitv<4>(); else waitv<0>();
}
__device__ __forceinline__ u32x4 pack2(unsigned a0, unsigned a1, unsigned b0, unsigned b1) {
  auto r0 = __builtin_amdgcn_permlane16_swap(a0, b0, false, false);
  auto r1 = __builtin_amdgcn_permlane16_swap(a1, b1, false, false);
  return (u32x4){r0[0], r1[0], r0[1], r1[1]};
}

struct FragS { s16x8 a[2][3]; s16x8 b[2][2]; s16x8 sp[2]; };
struct FragB { s16x8 a[4]; s16x8 b[4]; };
#define SGB __builtin_amdgcn_sched_group_barrier
#define SB0 __builtin_amdgcn_sched_barrier(0)

__global__ __launch_bounds__(512, 2) void fused_kernel(const u16* __restrict__ x, const u16* __restrict__ wqkv, const u16* __restrict__ wo,
                                                       const u16* __restrict__ w1, const u16* __restrict__ w2, const u16* __restrict__ w3,
                                                       const u16* __restrict__ w4, const u16* __restrict__ gw, u16* __restrict__ out, char* __restrict__ ws) {
  extern __shared__ __attribute__((aligned(16))) char lds[];
  const int tid = threadIdx.x, lane = tid & 63, w = __builtin_amdgcn_readfirstlane(tid >> 6);
  const int b = blockIdx.x, xg = b & 7, li = b >> 3;
  const int sstart = (55 * xg + 1) >> 1, scnt = ((55 * (xg + 1) + 1) >> 1) - sstart;
  const bool hasS = li < scnt;
  const int sj = sstart + li, sr = sj / 10, sc = sj - sr * 10;
  const bool hasQ = li >= 2;  const int qi = li - 2, qr = 2 * xg + qi / 15, qc = qi % 15;
  const bool hasG = li >= 4;  const int gi = li - 4, gr = 2 * xg + gi / 14, gc = gi % 14;
  const int aB = 8 * xg + (li >> 2), apar = li & 1, ahh = (li >> 1) & 1;
  const int cu = 4 * xg + (li - 28);
  const bool hasC = (li >= 28) && (cu < 27);
  const unsigned ldsbase = (unsigned)(size_t)(LAS char*)lds;
#define STAMP() do {} while (0)
  STAMP();

  if (hasC) {
    float cs[8];
#pragma unroll
    for (int e = 0; e < 8; e++) cs[e] = 0.f;
    const char* src = (const char*)w3 + (size_t)(w * 8 + (lane >> 3)) * (F_ * 2) + cu * 128 + (lane & 7) * 16;
    u32x4 v[10];
#pragma unroll
    for (int i = 0; i < 10; i++) v[i] = *(const u32x4*)(src + (size_t)i * 64 * (F_ * 2));
#pragma unroll
    for (int i = 0; i < 10; i++) {
#pragma unroll
      for (int e = 0; e < 4; e++) { cs[2 * e] += bflo(v[i][e]); cs[2 * e + 1] += bfhi(v[i][e]); }
    }
#pragma unroll
    for (int e = 0; e < 8; e++) { cs[e] += __shfl_xor(cs[e], 8); cs[e] += __shfl_xor(cs[e], 16); cs[e] += __shfl_xor(cs[e], 32); }
    if (lane < 8) {
      *(f32x4*)(lds + L_SCR + w * 256 + lane * 32) = (f32x4){cs[0], cs[1], cs[2], cs[3]};
      *(f32x4*)(lds + L_SCR + w * 256 + lane * 32 + 16) = (f32x4){cs[4], cs[5], cs[6], cs[7]};
    }
    __syncthreads();
    if (w == 0) {
      float t = 0.f;
#pragma unroll
      for (int k = 0; k < 8; k++) t += *(const float*)(lds + L_SCR + k * 256 + lane * 4);
      __hip_atomic_store((GAS unsigned*)(ws + WS_CS) + cu * 64 + lane, __float_as_uint(t), __ATOMIC_RELAXED, __HIP_MEMORY_SCOPE_AGENT);
      const u64 E0 = __hip_atomic_load((GAS u64*)(ws + WS_EPOCH), __ATOMIC_RELAXED, __HIP_MEMORY_SCOPE_AGENT) + EPOCH_STEP;
      waitv<0>();
      if (lane < 4) __hip_atomic_store((GAS u64*)(ws + WS_FC) + cu * 4 + lane, E0, __ATOMIC_RELAXED, __HIP_MEMORY_SCOPE_AGENT);
    }
  }

  if (w >= 4) {
    const int lw = w - 4;
    int n_iss = 0, n_bar = 0;
    int cnt1 = 0, cnt2 = 0;
    const rsrc_t rWS = mkr(ws, WS_DBG);
    auto sync_one = [&]() {
      const int ahead = n_iss - n_bar - 1;
      waitv_dyn(ahead >= 2 ? cnt1 + cnt2 : (ahead == 1 ? cnt1 : 0));
      bar(); n_bar++;
    };
    auto make_room = [&]() { while (n_bar + 1 < n_iss) sync_one(); };
    auto pushed = [&](int c) { cnt2 = cnt1; cnt1 = c; n_iss++; };
    auto dep_wait = [&](unsigned foff) {
      const u64 E = *(volatile LAS u64*)(lds + L_EP);
      const unsigned elo = (unsigned)E, ehi = (unsigned)(E >> 32);
      const bool none = foff == BIGOFF;
      for (unsigned spin = 0; spin < SPIN_CAP; spin++) {
        u32x4 f0, f1;
        asm volatile("buffer_load_dwordx4 %0, %2, %3, 0 offen sc1\n\tbuffer_load_dwordx4 %1, %2, %3, 0 offen offset:16 sc1\n\ts_waitcnt vmcnt(0)"
                     : "=&v"(f0), "=&v"(f1) : "v"(foff), "s"(rWS) : "memory");
        const bool ok = none || (f0.x == elo && f0.y == ehi && f0.z == elo && f0.w == ehi && f1.x == elo && f1.y == ehi && f1.z == elo && f1.w == ehi);
        if (__all(ok)) break;
        if (n_bar < n_iss) { bar(); n_bar++; }
        else __builtin_amdgcn_s_sleep(2);
      }
    };
    const unsigned swA = (((lane & 7) ^ (((lw & 1) << 2) + (lane >> 4))) * 16);
    const unsigned swB = (((lane & 7) ^ (((((lane >> 3) >> 1) & 1) << 2) | ((lw & 1) << 1))) * 16);
    auto stream = [&](auto BIGT, rsrc_t rA, unsigned abase, unsigned lda, rsrc_t rB, unsigned ldb, unsigned bcol, int nch, int rot, int mode) {
      constexpr bool BIG = decltype(BIGT)::value;
      constexpr int NA = BIG ? 4 : 3, NB = BIG ? 4 : 2;
      const unsigned voA = (lane >> 3) * lda + swA, voB = (lane >> 3) * ldb + swB;
      int kc = rot;
      for (int c = 0; c < nch; c++) {
        make_room();
        const unsigned sl = ldsbase + (n_iss & 3) * SLOT;
        int extra = 0;
        if (mode == 1) {
          const int col = kc * 64 + (swA >> 1);
          const int sh = col < 160 ? -1 : (col < 320 ? 1 : (col < 480 ? -2 : 2));
#pragma unroll
          for (int i = 0; i < NA; i++) {
            const int p = lw + 4 * i; const int row = (int)abase + 8 * p + (lane >> 3) + sh;
            const unsigned vo = row < 0 ? BIGOFF : (unsigned)row * lda + swA;
            dma_sc1(sl + p * 1024, vo, rA, kc * 128);
          }
        } else {
#pragma unroll
          for (int i = 0; i < NA; i++) { const int p = lw + 4 * i; dma_sc1(sl + p * 1024, voA, rA, (abase + 8 * p) * lda + kc * 128); }
        }
#pragma unroll
        for (int i = 0; i < NB; i++) { const int pp = lw + 4 * i; dma(sl + BOFF + pp * 1024, voB, rB, (kc * 64 + 8 * (pp & 7)) * ldb + bcol + (pp >> 3) * 128); }
        if (c == 0) {
          if (mode == 2 && lw < 2) { dma(ldsbase + L_GW + lw * 1024, lane * 16, mkr(gw, D_ * 2), lw * 1024); extra = 1; }
          if (mode == 3) {
            const int R = (int)abase + 2 * lane, rt = R / 96, rr = R - rt * 96;
            const unsigned vo = WS_ST + rt * (20 * 768) + rr * 8;
#pragma unroll
            for (int i = 0; i < 5; i++) { const int pi = lw + 4 * i; dma_sc1(ldsbase + L_ST + pi * 1024, vo, rWS, pi * 768); }
            extra = 5;
            if (lw == 0) { dma_sc1(ldsbase + L_CS, lane < 32 ? lane * 16 : BIGOFF, rWS, WS_CS + bcol * 2); extra = 6; }
          }
        }
        pushed(NA + NB + extra);
        kc = (kc + 1 == nch) ? 0 : kc + 1;
      }
    };
    using TS = std::integral_constant<bool, false>; using TB = std::integral_constant<bool, true>;
    const rsrc_t rX = mkr(x, S_ * D_ * 2);
    const int rot10 = (sc + 3 * sr) % 10;
    if (hasS) stream(TS{}, rX, sr * 96, D_ * 2, mkr(w1, D_ * D_ * 2), D_ * 2, sc * 128, 10, rot10, 2);
    STAMP();
    if (hasQ) stream(TB{}, rX, qr * 128, D_ * 2, mkr(wqkv, D_ * Q_ * 2), Q_ * 2, qc * 256, 10, (qc * 7 + qr * 5) % 10, 0);
    STAMP();
    while (n_bar < 1) sync_one();
    if (hasS) {
      const int rt = sr - 1 + (lane >> 4), ct = lane & 15;
      dep_wait((lane < 48 && rt >= 0 && rt < 22 && ct < 10) ? WS_FH + (rt * 16 + ct) * 32 : BIGOFF);
      STAMP();
      stream(TS{}, mkr(ws + WS_H, S_ * D_ * 2), sr * 96, D_ * 2, mkr(w2, D_ * D_ * 2), D_ * 2, sc * 128, 10, rot10, 1);
    }
    STAMP();
    {
      const int r0 = max(32 * aB - 14, 0) >> 7, r1 = (32 * aB + 31) >> 7;
      const int rt = r0 + (lane >> 4), ct = lane & 15;
      dep_wait((rt <= r1 && ct < 15) ? WS_FQ + (rt * 16 + ct) * 32 : BIGOFF);
      STAMP();
      const rsrc_t rQ = mkr(ws + WS_QKV, S_ * Q_ * 2);
      const int s0 = 32 * aB + apar;
#pragma unroll 1
      for (int c = 0; c < 2; c++) {
        make_room();
        const unsigned sl = ldsbase + (n_iss & 3) * SLOT;
        const int np = c == 0 ? 7 : 4, lim = c == 0 ? 1560 : 920;
#pragma unroll 1
        for (int i = 0; i < np; i++) {
          const int p = lw + 4 * i, e = p * 64 + lane, row = e / 40, ch = e - row * 40;
          int grow, sect;
          if (c == 0) { const bool isq = row < 16; grow = isq ? s0 + 2 * row : s0 - 14 + 2 * (row - 16); sect = isq ? 0 : 1280; }
          else { grow = s0 - 14 + 2 * row; sect = 2560; }
          const unsigned vo = (e < lim && grow >= 0) ? (unsigned)(grow * (Q_ * 2) + sect + ahh * 640 + ch * 16) : BIGOFF;
          dma_sc1(sl + p * 1024, vo, rQ, 0);
        }
        pushed(np);
      }
    }
    STAMP();
    if (hasG) {
      const int r0 = (gr * 128) / 96, r1 = (gr * 128 + 127) / 96;
      const int rt = r0 + (lane >> 4), ct = lane & 15;
      unsigned fo = (lane < 48 && rt <= r1 && ct < 10) ? WS_FX + (rt * 16 + ct) * 32 : BIGOFF;
      if (lane >= 48 && lane < 50 && 2 * gc + (lane - 48) < 27) fo = WS_FC + (2 * gc + (lane - 48)) * 32;
      dep_wait(fo);
      STAMP();
      stream(TB{}, mkr(ws + WS_X1, S_ * D_ * 2), gr * 128, D_ * 2, mkr(w3, D_ * F_ * 2), F_ * 2, gc * 256, 10, (gc * 7 + gr * 5) % 10, 3);
    }
    STAMP();
    if (hasS) {
      dep_wait((lane < 12 && 3 * sr + (lane >> 2) < 64) ? WS_FO + (3 * sr * 4 + lane) * 32 : BIGOFF);
      STAMP();
      stream(TS{}, mkr(ws + WS_O, S_ * D_ * 2), sr * 96, D_ * 2, mkr(wo, D_ * D_ * 2), D_ * 2, sc * 128, 10, rot10, 0);
      STAMP();
      const int r0 = (sr * 96) >> 7, r1 = min(sr * 96 + 95, S_ - 1) >> 7;
      const int rt = r0 + (lane >> 4), ct = lane & 15;
      dep_wait((rt <= r1 && ct < 14) ? WS_FG + (rt * 16 + ct) * 32 : BIGOFF);
      STAMP();
      stream(TS{}, mkr(ws + WS_H2, S_ * F_ * 2), sr * 96, F_ * 2, mkr(w4, F_ * D_ * 2), D_ * 2, sc * 128, 27, (sc * 5 + sr * 9) % 27, 0);
    }
    while (n_bar < n_iss) sync_one();
    STAMP();
  } else {
    const int wr = w >> 1, wc = w & 1;
    const int m16 = lane & 15, G = lane >> 4;
    const u64 E = __hip_atomic_load((GAS u64*)(ws + WS_EPOCH), __ATOMIC_RELAXED, __HIP_MEMORY_SCOPE_AGENT) + EPOCH_STEP;
    if (w == 3) {
      if (lane < 4) *(LAS unsigned*)(lds + L_ONE + lane * 4) = 0x3F803F80u;
      else if (lane < 8) *(LAS unsigned*)(lds + L_ONE + lane * 4) = 0u;
      if (lane == 0) *(LAS u64*)(lds + L_EP) = E;
      asm volatile("s_waitcnt lgkmcnt(0)" ::: "memory");
    }
    const rsrc_t rWS = mkr(ws, WS_DBG);
    int n = 0;
    auto publish = [&](unsigned foff) {
      waitv<0>();
      if (lane == 0) __hip_atomic_store((GAS u64*)(ws + foff) + w, E, __ATOMIC_RELAXED, __HIP_MEMORY_SCOPE_AGENT);
    };
    const int q4 = (lane & 15) >> 2, p4 = lane & 3;
    const int bx = (((q4 >> 1) & 1) << 2) | ((G & 1) << 1);
    const unsigned sA0 = (G ^ ((m16 >> 1) & 7)) * 16, sA1 = ((4 + G) ^ ((m16 >> 1) & 7)) * 16;
    const unsigned brow = BOFF + (8 * G + q4) * 128 + 8 * (p4 & 1);
    const int cst = (G & 1) * 16 + (G >> 1) * 8;

    const unsigned saoff0 = (wr * 48 + m16) * 128 + sA0, saoff1 = (wr * 48 + m16) * 128 + sA1;
    const unsigned sboff0 = brow + (((wc * 4 + 0 + (p4 >> 1)) ^ bx) * 16), sboff1 = brow + (((wc * 4 + 2 + (p4 >> 1)) ^ bx) * 16);
    f32x4 acc[3][2];
    f32x4 aS[3], aQ[3], aC[2];
    u32x2 xt[3][2];
    float gate[3];
    auto small = [&](auto NCHT, auto STATT, int rot) {
      constexpr int NCH = decltype(NCHT)::value; constexpr bool STAT = decltype(STATT)::value;
#pragma unroll
      for (int i = 0; i < 3; i++)
#pragma unroll
        for (int j = 0; j < 2; j++) acc[i][j] = (f32x4){0.f, 0.f, 0.f, 0.f};
      const s16x8 ones = {0x3F80, 0x3F80, 0x3F80, 0x3F80, 0x3F80, 0x3F80, 0x3F80, 0x3F80};
      unsigned spbase = 0, spstep = 0; int kc = rot;
      if (STAT) {
#pragma unroll
        for (int i = 0; i < 3; i++) { aS[i] = (f32x4){0.f, 0.f, 0.f, 0.f}; aQ[i] = (f32x4){0.f, 0.f, 0.f, 0.f}; }
        aC[0] = aC[1] = (f32x4){0.f, 0.f, 0.f, 0.f};
        const int c3 = m16 & 3;
        spbase = c3 == 1 ? L_GW + G * 16 : (c3 == 0 ? L_ONE : L_ONE + 16);
        spstep = c3 == 1 ? 64 : 0;
      }
      auto rd = [&](FragS& Fr) {
        const char* sb = lds + (n & 3) * SLOT;
#pragma unroll
        for (int ks = 0; ks < 2; ks++) {
          s16x4 l0 = __builtin_amdgcn_ds_read_tr16_b64_v4i16((LAS s16x4*)(sb + sboff0 + ks * 4096));
          s16x4 h0 = __builtin_amdgcn_ds_read_tr16_b64_v4i16((LAS s16x4*)(sb + sboff0 + ks * 4096 + 512));
          s16x4 l1 = __builtin_amdgcn_ds_read_tr16_b64_v4i16((LAS s16x4*)(sb + sboff1 + ks * 4096));
          s16x4 h1 = __builtin_amdgcn_ds_read_tr16_b64_v4i16((LAS s16x4*)(sb + sboff1 + ks * 4096 + 512));
          Fr.b[ks][0] = __builtin_shufflevector(l0, h0, 0, 1, 2, 3, 4, 5, 6, 7);
          Fr.b[ks][1] = __builtin_shufflevector(l1, h1, 0, 1, 2, 3, 4, 5, 6, 7);
#pragma unroll
          for (int i = 0; i < 3; i++) Fr.a[ks][i] = *(const s16x8*)(sb + (ks ? saoff1 : saoff0) + i * 2048);
          if (STAT) Fr.sp[ks] = *(const s16x8*)(lds + spbase + spstep * (2 * kc + ks));
        }
        if (STAT) {
          if (kc == sc) {
#pragma unroll
            for (int i = 0; i < 3; i++)
#pragma unroll
              for (int j = 0; j < 2; j++)
                xt[i][j] = *(const u32x2*)(sb + (wr * 48 + 16 * i + m16) * 128 + (((wc * 4 + 2 * j + (G >> 1)) ^ ((m16 >> 1) & 7)) * 16) + (G & 1) * 8);
          }
          kc = (kc + 1 == NCH) ? 0 : kc + 1;
        }
        n++;
      };
      auto mm = [&](FragS& Fr) {
#pragma unroll
        for (int ks = 0; ks < 2; ks++) {
#pragma unroll
          for (int i = 0; i < 3; i++)
#pragma unroll
            for (int j = 0; j < 2; j++) acc[i][j] = __builtin_amdgcn_mfma_f32_16x16x32_bf16(Fr.b[ks][j], Fr.a[ks][i], acc[i][j], 0, 0, 0);
          if (STAT) {
#pragma unroll
            for (int i = 0; i < 3; i++) {
              aS[i] = __builtin_amdgcn_mfma_f32_16x16x32_bf16(Fr.sp[ks], Fr.a[ks][i], aS[i], 0, 0, 0);
              aQ[i] = __builtin_amdgcn_mfma_f32_16x16x32_bf16(Fr.a[ks][i], Fr.a[ks][i], aQ[i], 0, 0, 0);
            }
#pragma unroll
            for (int j = 0; j < 2; j++) aC[j] = __builtin_amdgcn_mfma_f32_16x16x32_bf16(Fr.b[ks][j], ones, aC[j], 0, 0, 0);
          }
        }
      };
      FragS F0, F1;
      bar(); SB0;
      rd(F0); SB0;
#pragma unroll 1
      for (int c = 1; c + 1 < NCH; c += 2) {
        bar(); SB0; rd(F1); mm(F0); SB0;
        bar(); SB0; rd(F0); mm(F1); SB0;
      }
      if (NCH % 2 == 0) { bar(); SB0; rd(F1); mm(F0); SB0; mm(F1); }
      else mm(F0);
      SB0;
    };
    f32x4 bacc[4][4];
    auto big = [&]() {
#pragma unroll
      for (int i = 0; i < 4; i++)
#pragma unroll
        for (int j = 0; j < 4; j++) bacc[i][j] = (f32x4){0.f, 0.f, 0.f, 0.f};
      const unsigned a0 = (wr * 64 + m16) * 128 + sA0, a1 = (wr * 64 + m16) * 128 + sA1;
      unsigned bo[4];
#pragma unroll
      for (int j = 0; j < 4; j++) bo[j] = brow + wc * 8192 + (((2 * j + (p4 >> 1)) ^ bx) * 16);
      auto rd = [&](FragB& Fr, int ks) {
        const char* sb = lds + (n & 3) * SLOT;
#pragma unroll
        for (int j = 0; j < 4; j++) {
          s16x4 lo = __builtin_amdgcn_ds_read_tr16_b64_v4i16((LAS s16x4*)(sb + bo[j] + ks * 4096));
          s16x4 hi = __builtin_amdgcn_ds_read_tr16_b64_v4i16((LAS s16x4*)(sb + bo[j] + ks * 4096 + 512));
          Fr.b[j] = __builtin_shufflevector(lo, hi, 0, 1, 2, 3, 4, 5, 6, 7);
        }
#pragma unroll
        for (int i = 0; i < 4; i++) Fr.a[i] = *(const s16x8*)(sb + (ks ? a1 : a0) + i * 2048);
      };
      auto mm = [&](FragB& Fr) {
#pragma unroll
        for (int i = 0; i < 4; i++)
#pragma unroll
          for (int j = 0; j < 4; j++) bacc[i][j] = __builtin_amdgcn_mfma_f32_16x16x32_bf16(Fr.b[j], Fr.a[i], bacc[i][j], 0, 0, 0);
      };
      auto sched = [&]() {
#pragma unroll
        for (int i = 0; i < 12; i++) { SGB(0x8, 1, 0); SGB(0x100, 1, 0); }
        SGB(0x8, 4, 0);
      };
      FragB F0, F1;
      bar(); SB0;
      rd(F0, 0); SB0;
#pragma unroll 1
      for (int c = 0; c < 9; c++) {
        rd(F1, 1); mm(F0); sched(); SB0;
        bar(); n++; SB0;
        rd(F0, 0); mm(F1); sched(); SB0;
      }
      rd(F1, 1); mm(F0); sched(); SB0;
      mm(F1); n++; SB0;
    };
    using I10 = std::integral_constant<int, 10>; using I27 = std::integral_constant<int, 27>;
    using TT = std::integral_constant<bool, true>; using TF = std::integral_constant<bool, false>;
    const int rot10 = (sc + 3 * sr) % 10;
    const unsigned srow = sr * 96 + wr * 48 + m16;
    const unsigned scolb = (sc * 64 + wc * 32 + cst) * 2;

    if (hasS) {
      small(I10{}, TT{}, rot10);
      STAMP();
      const rsrc_t rH = mkr(ws + WS_H, S_ * D_ * 2);
#pragma unroll
      for (int i = 0; i < 3; i++) {
        const float mean = aS[i][0] * (1.0f / D_);
        const int r = m16 & 3;
        float q = r == 0 ? aQ[i][0] : (r == 1 ? aQ[i][1] : (r == 2 ? aQ[i][2] : aQ[i][3]));
        q = __shfl(q, m16 + 16 * (m16 >> 2));
        const float var = fmaxf(q * (1.0f / D_) - mean * mean, 0.f);
        const float rstd = __builtin_amdgcn_rsqf(var + 1e-5f);
        gate[i] = __builtin_amdgcn_rcpf(1.0f + __builtin_amdgcn_exp2f(-1.4426950408889634f * aS[i][1]));
        unsigned pk[2][2];
#pragma unroll
        for (int j = 0; j < 2; j++) {
          float v[4];
#pragma unroll
          for (int e = 0; e < 4; e++) v[e] = gelu_t((acc[i][j][e] - mean * aC[j][e]) * rstd);
          pk[j][0] = cvt_pk(v[0], v[1]); pk[j][1] = cvt_pk(v[2], v[3]);
        }
        __builtin_amdgcn_raw_buffer_store_b128(pack2(pk[0][0], pk[0][1], pk[1][0], pk[1][1]), rH, (srow + 16 * i) * (D_ * 2) + scolb, 0, 16);
      }
      publish(WS_FH + (sr * 16 + sc) * 32);
    }
    STAMP();
    if (hasQ) {
      big();
      STAMP();
      const rsrc_t rQ = mkr(ws + WS_QKV, S_ * Q_ * 2);
#pragma unroll
      for (int i = 0; i < 4; i++)
#pragma unroll
        for (int jp = 0; jp < 2; jp++) {
          const f32x4 u = bacc[i][2 * jp], v = bacc[i][2 * jp + 1];
          __builtin_amdgcn_raw_buffer_store_b128(pack2(cvt_pk(u[0], u[1]), cvt_pk(u[2], u[3]), cvt_pk(v[0], v[1]), cvt_pk(v[2], v[3])), rQ,
                                                 (qr * 128 + wr * 64 + 16 * i + m16) * (Q_ * 2) + (qc * 128 + wc * 64 + jp * 32 + cst) * 2, 0, 16);
        }
      publish(WS_FQ + (qr * 16 + qc) * 32);
    }
    STAMP();
    if (hasS) {
      small(I10{}, TF{}, rot10);
      STAMP();
      const rsrc_t rX1 = mkr(ws + WS_X1, S_ * D_ * 2);
      float ssum = 0.f, ssq = 0.f;
#pragma unroll
      for (int i = 0; i < 3; i++) {
        float s = 0.f, q = 0.f;
#pragma unroll
        for (int j = 0; j < 2; j++) {
          const unsigned p0 = cvt_pk(bflo(xt[i][j].x) + acc[i][j][0], bfhi(xt[i][j].x) + acc[i][j][1]);
          const unsigned p1 = cvt_pk(bflo(xt[i][j].y) + acc[i][j][2], bfhi(xt[i][j].y) + acc[i][j][3]);
          xt[i][j] = (u32x2){p0, p1};
          s = dot2(p0, 0x3F803F80u, s); s = dot2(p1, 0x3F803F80u, s);
          q = dot2(p0, p0, q); q = dot2(p1, p1, q);
        }
        __builtin_amdgcn_raw_buffer_store_b128(pack2(xt[i][0].x, xt[i][0].y, xt[i][1].x, xt[i][1].y), rX1, (srow + 16 * i) * (D_ * 2) + scolb, 0, 16);
        s += __shfl_xor(s, 16); s += __shfl_xor(s, 32);
        q += __shfl_xor(q, 16); q += __shfl_xor(q, 32);
        if (G == i) { ssum = s; ssq = q; }
      }
      if (G < 3) {
        u32x2 sv = {__float_as_uint(ssum), __float_as_uint(ssq)};
        __builtin_amdgcn_raw_buffer_store_b64(sv, rWS, WS_ST + ((sr * 20 + sc * 2 + wc) * 96 + wr * 48 + 16 * G + m16) * 8, 0, 16);
      }
      publish(WS_FX + (sr * 16 + sc) * 32);
    }
    STAMP();
    {
      const int hl = lane & 15, ri = w * 4 + (lane >> 4);
      char* s0p = lds + (n & 3) * SLOT;
      const char* s1p = lds + ((n + 1) & 3) * SLOT;
      bar(); SB0;
      unsigned qv[10];
      {
        const char* qp = s0p + ri * 640 + hl * 40;
#pragma unroll
        for (int d = 0; d < 5; d++) { const u32x2 t = *(const u32x2*)(qp + d * 8); qv[2 * d] = t.x; qv[2 * d + 1] = t.y; }
      }
      float lg[8];
      const int srow_g = 32 * aB + apar + 2 * ri;
#pragma unroll
      for (int ww = 0; ww < 8; ww++) {
        const char* kp = s0p + (16 + ri + 7 - ww) * 640 + hl * 40;
        float a0 = 0.f, a1 = 0.f;
#pragma unroll
        for (int d = 0; d < 5; d++) { const u32x2 t = *(const u32x2*)(kp + d * 8); a0 = dot2(qv[2 * d], t.x, a0); a1 = dot2(qv[2 * d + 1], t.y, a1); }
        lg[ww] = (srow_g - 2 * ww >= 0) ? (a0 + a1) * (0.22360679774997896f * 1.4426950408889634f) : -1e30f;
      }
      float mx = lg[0];
#pragma unroll
      for (int ww = 1; ww < 8; ww++) mx = fmaxf(mx, lg[ww]);
      float sum = 0.f;
#pragma unroll
      for (int ww = 0; ww < 8; ww++) { lg[ww] = __builtin_amdgcn_exp2f(lg[ww] - mx); sum += lg[ww]; }
      const float inv = __builtin_amdgcn_rcpf(sum);
      unsigned pp[4];
#pragma unroll
      for (int k = 0; k < 4; k++) pp[k] = cvt_pk(lg[2 * k] * inv, lg[2 * k + 1] * inv);
      SB0; bar(); SB0;
      float o[20];
#pragma unroll
      for (int d = 0; d < 20; d++) o[d] = 0.f;
#pragma unroll
      for (int k = 0; k < 4; k++) {
        const char* va = s1p + (ri + 7 - 2 * k) * 640 + hl * 40;
        const char* vb = va - 640;
#pragma unroll
        for (int d = 0; d < 5; d++) {
          const u32x2 ta = *(const u32x2*)(va + d * 8), tb = *(const u32x2*)(vb + d * 8);
          o[4 * d + 0] = dot2(__builtin_amdgcn_perm(tb.x, ta.x, 0x05040100u), pp[k], o[4 * d + 0]);
          o[4 * d + 1] = dot2(__builtin_amdgcn_perm(tb.x, ta.x, 0x07060302u), pp[k], o[4 * d + 1]);
          o[4 * d + 2] = dot2(__builtin_amdgcn_perm(tb.y, ta.y, 0x05040100u), pp[k], o[4 * d + 2]);
          o[4 * d + 3] = dot2(__builtin_amdgcn_perm(tb.y, ta.y, 0x07060302u), pp[k], o[4 * d + 3]);
        }
      }
      {
        char* op = s0p + ri * 640 + hl * 40;
#pragma unroll
        for (int d = 0; d < 5; d++) *(u32x2*)(op + d * 8) = (u32x2){cvt_pk(o[4 * d], o[4 * d + 1]), cvt_pk(o[4 * d + 2], o[4 * d + 3])};
      }
      asm volatile("s_waitcnt lgkmcnt(0)" ::: "memory");
      const rsrc_t rO = mkr(ws + WS_O, S_ * D_ * 2);
#pragma unroll
      for (int t = 0; t < 3; t++) {
        const int e = t * 64 + lane, rw = e / 40, ch = e - rw * 40;
        if (e < 160) {
          const u32x4 v = *(const u32x4*)(s0p + w * 2560 + e * 16);
          __builtin_amdgcn_raw_buffer_store_b128(v, rO, (32 * aB + apar + 2 * (w * 4 + rw)) * (D_ * 2) + ahh * 640 + ch * 16, 0, 16);
        }
      }
      publish(WS_FO + (aB * 4 + (li & 3)) * 32);
      n += 2;
    }
    STAMP();
    if (hasG) {
      big();
      STAMP();
      float s = 0.f, q = 0.f;
#pragma unroll
      for (int pi = 0; pi < 20; pi++) { const f32x2 t = *(const f32x2*)(lds + L_ST + pi * 1024 + (wr * 64 + lane) * 8); s += t.x; q += t.y; }
      const float mean_l = s * (1.0f / D_);
      const float rstd_l = __builtin_amdgcn_rsqf(fmaxf(q * (1.0f / D_) - mean_l * mean_l, 0.f) + 1e-5f);
      f32x4 cs[4];
#pragma unroll
      for (int j = 0; j < 4; j++) cs[j] = *(const f32x4*)(lds + L_CS + (wc * 64 + 16 * j + 4 * G) * 4);
      const rsrc_t rG = mkr(ws + WS_H2, S_ * F_ * 2);
      const bool colok = gc * 128 + wc * 64 < F_;
#pragma unroll
      for (int i = 0; i < 4; i++) {
        const float mean = __shfl(mean_l, 16 * i + m16), rstd = __shfl(rstd_l, 16 * i + m16);
        unsigned pk[4][2];
#pragma unroll
        for (int j = 0; j < 4; j++) {
          float v[4];
#pragma unroll
          for (int e = 0; e < 4; e++) v[e] = gelu_t((bacc[i][j][e] - mean * cs[j][e]) * rstd);
          pk[j][0] = cvt_pk(v[0], v[1]); pk[j][1] = cvt_pk(v[2], v[3]);
        }
        if (colok) {
#pragma unroll
          for (int jp = 0; jp < 2; jp++)
            __builtin_amdgcn_raw_buffer_store_b128(pack2(pk[2 * jp][0], pk[2 * jp][1], pk[2 * jp + 1][0], pk[2 * jp + 1][1]), rG,
                                                   (gr * 128 + wr * 64 + 16 * i + m16) * (F_ * 2) + (gc * 128 + wc * 64 + jp * 32 + cst) * 2, 0, 16);
        }
      }
      publish(WS_FG + (gr * 16 + gc) * 32);
    }
    STAMP();
    if (hasS) {
      small(I10{}, TF{}, rot10);
      STAMP();
      f32x4 tacc[3][2];
#pragma unroll
      for (int i = 0; i < 3; i++)
#pragma unroll
        for (int j = 0; j < 2; j++) {
          const float g = gate[i], h = 1.0f - g;
          tacc[i][j] = (f32x4){g * acc[i][j][0] + h * bflo(xt[i][j].x), g * acc[i][j][1] + h * bfhi(xt[i][j].x),
                               g * acc[i][j][2] + h * bflo(xt[i][j].y), g * acc[i][j][3] + h * bfhi(xt[i][j].y)};
        }
      small(I27{}, TF{}, 0);
      STAMP();
      const rsrc_t rOut = mkr(out, S_ * D_ * 2);
#pragma unroll
      for (int i = 0; i < 3; i++) {
        const float h = 1.0f - gate[i];
        unsigned pk[2][2];
#pragma unroll
        for (int j = 0; j < 2; j++) {
          pk[j][0] = cvt_pk(tacc[i][j][0] + h * acc[i][j][0], tacc[i][j][1] + h * acc[i][j][1]);
          pk[j][1] = cvt_pk(tacc[i][j][2] + h * acc[i][j][2], tacc[i][j][3] + h * acc[i][j][3]);
        }
        __builtin_amdgcn_raw_buffer_store_b128(pack2(pk[0][0], pk[0][1], pk[1][0], pk[1][1]), rOut, (srow + 16 * i) * (D_ * 2) + scolb, 0, 0);
      }
    }
    if (b == 255 && w == 0) {
      const unsigned elo = (unsigned)E, ehi = (unsigned)(E >> 32);
      for (unsigned spin = 0; spin < SPIN_CAP; spin++) {
        bool ok = true;
#pragma unroll
        for (int t = 0; t < 8; t++) {
          const u32x4 f = __builtin_amdgcn_raw_buffer_load_b128(rWS, WS_FO + (t * 64 + lane) * 16, 0, 16);
          ok = ok && f.x == elo && f.y == ehi && f.z == elo && f.w == ehi;
        }
        if (__all(ok)) break;
        __builtin_amdgcn_s_sleep(4);
      }
      if (lane == 0) __hip_atomic_store((GAS u64*)(ws + WS_EPOCH), E, __ATOMIC_RELAXED, __HIP_MEMORY_SCOPE_AGENT);
    }
    STAMP();
  }
}

extern "C" void kernel_launch(void* const* d_in, const int* in_sizes, int n_in, void* d_out, int out_size, void* d_ws, size_t ws_size,
                              hipStream_t stream) {
  (void)in_sizes; (void)n_in; (void)out_size; (void)ws_size;
  (void)hipFuncSetAttribute((const void*)fused_kernel, hipFuncAttributeMaxDynamicSharedMemorySize, LDS_BYTES);
  fused_kernel<<<256, 512, LDS_BYTES, stream>>>((const u16*)d_in[0], (const u16*)d_in[1], (const u16*)d_in[2], (const u16*)d_in[4],
                                                (const u16*)d_in[5], (const u16*)d_in[6], (const u16*)d_in[7], (const u16*)d_in[8],
                                                (u16*)d_out, (char*)d_ws);
}
```

```cpp
#include <hip/hip_runtime.h>
#include <hip/hip_bf16.h>

typedef unsigned short u16;
typedef unsigned long long u64;
typedef unsigned u32x4 __attribute__((ext_vector_type(4)));
typedef unsigned u32x2 __attribute__((ext_vector_type(2)));
typedef short s16x4 __attribute__((ext_vector_type(4)));
typedef short s16x8 __attribute__((ext_vector_type(8)));
typedef float f32x4 __attribute__((ext_vector_type(4)));
typedef float f32x2 __attribute__((ext_vector_type(2)));
typedef __bf16 bf16x2_t __attribute__((ext_vector_type(2)));
#define LAS __attribute__((address_space(3)))
#define GAS __attribute__((address_space(1)))
typedef __amdgpu_buffer_rsrc_t rsrc_t;

#define DBG_STAMPS 0

constexpr int S_ = 2048, D_ = 640, F_ = 1728, Q_ = 1920;
constexpr unsigned WS_EPOCH = 0;
constexpr unsigned WS_FH   = 4096;
constexpr unsigned WS_FQ   = WS_FH + 16384;
constexpr unsigned WS_FX   = WS_FQ + 16384;
constexpr unsigned WS_FO   = WS_FX + 16384;
constexpr unsigned WS_FG   = WS_FO + 16384;
constexpr unsigned WS_FC   = WS_FG + 16384;
constexpr unsigned WS_FLAGS_END = WS_FC + 16384;
constexpr unsigned WS_H    = 131072;
constexpr unsigned WS_QKV  = WS_H + S_ * D_ * 2;
constexpr unsigned WS_X1   = WS_QKV + S_ * Q_ * 2;
constexpr unsigned WS_O    = WS_X1 + S_ * D_ * 2;
constexpr unsigned WS_H2   = WS_O + S_ * D_ * 2;
constexpr unsigned WS_ST   = WS_H2 + S_ * F_ * 2;
constexpr unsigned ST_BYTES = 22 * 20 * 96 * 8;
constexpr unsigned WS_CS   = WS_ST + ST_BYTES;
constexpr unsigned WS_DBG  = WS_CS + 8192;
constexpr unsigned SLOT = 32768, BOFF = 16384;
constexpr unsigned L_ST = 131072, L_CS = L_ST + 20480, L_GW = L_CS + 1024, L_ONE = L_GW + 2048, L_EP = L_ONE + 64, L_SCR = L_EP + 64;
constexpr unsigned L_POLL = L_SCR + 2048, L_DUM = L_POLL + 2048;
constexpr unsigned LDS_BYTES = L_DUM + 1024;
constexpr u64 EPOCH_STEP = 0x9E3779B97F4A7C15ull;
constexpr unsigned BIGOFF = 0x7fffff00u;
constexpr unsigned SPIN_CAP = 1u << 17;

template <int N> __device__ __forceinline__ void waitv() { asm volatile("s_waitcnt vmcnt(%0)" ::"i"(N) : "memory"); }
__device__ __forceinline__ void bar() { asm volatile("" ::: "memory"); __builtin_amdgcn_s_barrier(); asm volatile("" ::: "memory"); }
__device__ __forceinline__ unsigned cvt_pk(float lo, float hi) { f32x2 f = {lo, hi}; bf16x2_t b = __builtin_convertvector(f, bf16x2_t); return __builtin_bit_cast(unsigned, b); }
__device__ __forceinline__ float bflo(unsigned v) { return __uint_as_float(v << 16); }
__device__ __forceinline__ float bfhi(unsigned v) { return __uint_as_float(v & 0xffff0000u); }
__device__ __forceinline__ float dot2(unsigned a, unsigned b, float c) { return __builtin_amdgcn_fdot2_f32_bf16(__builtin_bit_cast(bf16x2_t, a), __builtin_bit_cast(bf16x2_t, b), c, false); }
__device__ __forceinline__ rsrc_t mkr(const void* p, unsigned bytes) { return __builtin_amdgcn_make_buffer_rsrc((void*)p, (short)0, (int)bytes, 0x00020000); }
__device__ __forceinline__ void dma(unsigned m0v, unsigned vo, rsrc_t r, unsigned so) {
  asm volatile("s_mov_b32 m0, %0\n\ts_nop 3\n\tbuffer_load_dwordx4 %1, %2, %3 offen lds" ::"s"(m0v), "v"(vo), "s"(r), "s"(so) : "memory");
}
__device__ __forceinline__ void dma_sc1(unsigned m0v, unsigned vo, rsrc_t r, unsigned so) {
  asm volatile("s_mov_b32 m0, %0\n\ts_nop 3\n\tbuffer_load_dwordx4 %1, %2, %3 offen sc1 lds" ::"s"(m0v), "v"(vo), "s"(r), "s"(so) : "memory");
}
__device__ __forceinline__ float gelu_t(float x) {
  const float k1 = -2.0f * 0.7978845608028654f * 1.4426950408889634f, k2 = k1 * 0.044715f;
  float t = x * __builtin_fmaf(x * x, k2, k1);
  float e = __builtin_amdgcn_exp2f(t);
  return x * __builtin_amdgcn_rcpf(1.0f + e);
}
__device__ __forceinline__ void dma2(unsigned m0v, unsigned v0, unsigned v1, rsrc_t r, unsigned so) {
  asm volatile("s_mov_b32 m0, %0\n\ts_nop 3\n\tbuffer_load_dwordx4 %1, %3, %4 offen lds\n\tbuffer_load_dwordx4 %2, %3, %4 offen offset:1024 lds"
               ::"s"(m0v), "v"(v0), "v"(v1), "s"(r), "s"(so) : "memory");
}
__device__ __forceinline__ void dma4(unsigned m0v, unsigned v0, unsigned v1, unsigned v2, unsigned v3, rsrc_t r, unsigned so) {
  asm volatile("s_mov_b32 m0, %0\n\ts_nop 3\n\tbuffer_load_dwordx4 %1, %5, %6 offen lds\n\tbuffer_load_dwordx4 %2, %5, %6 offen offset:1024 lds\n\t"
               "buffer_load_dwordx4 %3, %5, %6 offen offset:2048 lds\n\tbuffer_load_dwordx4 %4, %5, %6 offen offset:3072 lds"
               ::"s"(m0v), "v"(v0), "v"(v1), "v"(v2), "v"(v3), "s"(r), "s"(so) : "memory");
}
__device__ __forceinline__ void dma3s(unsigned m0v, unsigned v0, unsigned v1, unsigned v2, rsrc_t r, unsigned so) {
  asm volatile("s_mov_b32 m0, %0\n\ts_nop 3\n\tbuffer_load_dwordx4 %1, %4, %5 offen sc1 lds\n\tbuffer_load_dwordx4 %2, %4, %5 offen offset:1024 sc1 lds\n\t"
               "buffer_load_dwordx4 %3, %4, %5 offen offset:2048 sc1 lds"
               ::"s"(m0v), "v"(v0), "v"(v1), "v"(v2), "s"(r), "s"(so) : "memory");
}
__device__ __forceinline__ void dma4s(unsigned m0v, unsigned v0, unsigned v1, unsigned v2, unsigned v3, rsrc_t r, unsigned so) {
  asm volatile("s_mov_b32 m0, %0\n\ts_nop 3\n\tbuffer_load_dwordx4 %1, %5, %6 offen sc1 lds\n\tbuffer_load_dwordx4 %2, %5, %6 offen offset:1024 sc1 lds\n\t"
               "buffer_load_dwordx4 %3, %5, %6 offen offset:2048 sc1 lds\n\tbuffer_load_dwordx4 %4, %5, %6 offen offset:3072 sc1 lds"
               ::"s"(m0v), "v"(v0), "v"(v1), "v"(v2), "v"(v3), "s"(r), "s"(so) : "memory");
}
__device__ __forceinline__ void touch(unsigned m0v, unsigned vo, rsrc_t r, unsigned so) {
  asm volatile("s_mov_b32 m0, %0\n\ts_nop 3\n\tbuffer_load_dword %1, %2, %3 offen lds" ::"s"(m0v), "v"(vo), "s"(r), "s"(so) : "memory");
}
__device__ __forceinline__ u32x4 pack2(unsigned a0, unsigned a1, unsigned b0, unsigned b1) {
  auto r0 = __builtin_amdgcn_permlane16_swap(a0, b0, false, false);
  auto r1 = __builtin_amdgcn_permlane16_swap(a1, b1, false, false);
  return (u32x4){r0[0], r1[0], r0[1], r1[1]};
}

struct FragS { s16x8 a[2][3]; s16x8 b[2][2]; s16x8 sp[2]; };
struct FragB { s16x8 a[4]; s16x8 b[4]; };
#define SGB __builtin_amdgcn_sched_group_barrier
#define SB0 __builtin_amdgcn_sched_barrier(0)
#define LG0 __builtin_amdgcn_s_waitcnt(0xC07F)

__global__ __launch_bounds__(512, 2) void fused_kernel(const u16* __restrict__ x, const u16* __restrict__ wqkv, const u16* __restrict__ wo,
                                                       const u16* __restrict__ w1, const u16* __restrict__ w2, const u16* __restrict__ w3,
                                                       const u16* __restrict__ w4, const u16* __restrict__ gw, u16* __restrict__ out, char* __restrict__ ws) {
  extern __shared__ __attribute__((aligned(16))) char lds[];
  const int tid = threadIdx.x, lane = tid & 63, w = __builtin_amdgcn_readfirstlane(tid >> 6);
  const int b = blockIdx.x, xg = b & 7, li = b >> 3;
  const int sstart = (55 * xg + 1) >> 1, scnt = ((55 * (xg + 1) + 1) >> 1) - sstart;
  const bool hasS = li < scnt;
  const int sj = sstart + li, sr = sj / 10, sc = sj - sr * 10;
  const bool hasQ = li >= 2;  const int qi = li - 2, qr = 2 * xg + qi / 15, qc = qi % 15;
  const bool hasG = li >= 4;  const int gi = li - 4, gr = 2 * xg + gi / 14, gc = gi % 14;
  const int aB = 8 * xg + (li >> 2), apar = li & 1, ahh = (li >> 1) & 1;
  const int cu = 4 * xg + (li - 28);
  const bool hasC = (li >= 28) && (cu < 27);
  const unsigned ldsbase = (unsigned)(size_t)(LAS char*)lds;
#define STAMP() do {} while (0)
  STAMP();

  if (hasC) {
    float cs[8];
#pragma unroll
    for (int e = 0; e < 8; e++) cs[e] = 0.f;
    const char* src = (const char*)w3 + (size_t)(w * 8 + (lane >> 3)) * (F_ * 2) + cu * 128 + (lane & 7) * 16;
    u32x4 v[10];
#pragma unroll
    for (int i = 0; i < 10; i++) v[i] = *(const u32x4*)(src + (size_t)i * 64 * (F_ * 2));
#pragma unroll
    for (int i = 0; i < 10; i++) {
#pragma unroll
      for (int e = 0; e < 4; e++) { cs[2 * e] += bflo(v[i][e]); cs[2 * e + 1] += bfhi(v[i][e]); }
    }
#pragma unroll
    for (int e = 0; e < 8; e++) { cs[e] += __shfl_xor(cs[e], 8); cs[e] += __shfl_xor(cs[e], 16); cs[e] += __shfl_xor(cs[e], 32); }
    if (lane < 8) {
      *(f32x4*)(lds + L_SCR + w * 256 + lane * 32) = (f32x4){cs[0], cs[1], cs[2], cs[3]};
      *(f32x4*)(lds + L_SCR + w * 256 + lane * 32 + 16) = (f32x4){cs[4], cs[5], cs[6], cs[7]};
    }
    __syncthreads();
    if (w == 0) {
      float t = 0.f;
#pragma unroll
      for (int k = 0; k < 8; k++) t += *(const float*)(lds + L_SCR + k * 256 + lane * 4);
      __hip_atomic_store((GAS unsigned*)(ws + WS_CS) + cu * 64 + lane, __float_as_uint(t), __ATOMIC_RELAXED, __HIP_MEMORY_SCOPE_AGENT);
      const u64 E0 = __hip_atomic_load((GAS u64*)(ws + WS_EPOCH), __ATOMIC_RELAXED, __HIP_MEMORY_SCOPE_AGENT) + EPOCH_STEP;
      waitv<0>();
      if (lane < 4) __hip_atomic_store((GAS u64*)(ws + WS_FC) + cu * 4 + lane, E0, __ATOMIC_RELAXED, __HIP_MEMORY_SCOPE_AGENT);
    }
  }

  if (w >= 4) {
    const int lw = w - 4;
    unsigned slot = 0;
#define TRACE() do {} while (0)
#define PIN(v) asm volatile("" : "+v"(v))
    auto nexts = [&]() { slot = (slot + SLOT) & (4 * SLOT - 1); };
    const int l3 = lane >> 3;
    auto voffA = [&](int p, int i, unsigned ld) { return (8 * p + l3) * ld + (((lane & 7) ^ (((p & 1) << 2) + (lane >> 4))) * 16) - i * 1024; };
    auto voffB = [&](int pp, int i, unsigned ld) { const int ii = pp & 7;
      return (8 * ii + l3) * ld + (pp >> 3) * 128 + (((lane & 7) ^ ((((l3 >> 1) & 1) << 2) | ((ii & 1) << 1))) * 16) - i * 1024; };
    unsigned sa[3], sb2[2];
#pragma unroll
    for (int i = 0; i < 3; i++) sa[i] = voffA(3 * lw + i, i, D_ * 2);
#pragma unroll
    for (int i = 0; i < 2; i++) sb2[i] = voffB(2 * lw + i, i, D_ * 2);
    const unsigned mAs = ldsbase + 3 * lw * 1024, mBs = ldsbase + BOFF + 2 * lw * 1024;
    const unsigned mAb = ldsbase + 4 * lw * 1024, mBb = ldsbase + BOFF + 4 * lw * 1024;
    const unsigned t50 = lane < 50 ? lane * 128 : BIGOFF, t45 = lane < 45 ? lane * 128 : BIGOFF;
    const unsigned wq = li * 4 + lw;
    const rsrc_t rX = mkr(x, S_ * D_ * 2);
    const rsrc_t rWS = mkr(ws, WS_DBG);
    const unsigned sAo = sr * 96 * (D_ * 2), sBo = sc * 128;
    int kc;
    const rsrc_t rW1 = mkr(w1, D_ * D_ * 2);
    touch(ldsbase + L_DUM, t50, rW1, wq * 50 * 128);
    touch(ldsbase + L_DUM, lane < 20 ? lane * 128 : BIGOFF, rX, (xg * 2560 + wq * 20) * 128);
    auto issS = [&](rsrc_t rA, rsrc_t rB, int nch) {
      dma3s(mAs + slot, sa[0], sa[1], sa[2], rA, sAo + kc * 128);
      dma2(mBs + slot, sb2[0], sb2[1], rB, sBo + kc * (64 * D_ * 2));
      kc = (kc + 1 == nch) ? 0 : kc + 1; nexts();
    };
    if (hasS) {
      kc = sc;
      if (lw < 2) dma(ldsbase + L_GW + lw * 1024, lane * 16, mkr(gw, D_ * 2), lw * 1024);
      issS(rX, rW1, 10); issS(rX, rW1, 10);
    }
    STAMP();
    unsigned ba[4], bq[4], bg[4], ha[3], pv[5][2], at0[7], at1[4], stv, lc0, lc1;
#pragma unroll
    for (int i = 0; i < 4; i++) { ba[i] = voffA(4 * lw + i, i, D_ * 2); bq[i] = voffB(4 * lw + i, i, Q_ * 2); bg[i] = voffB(4 * lw + i, i, F_ * 2); PIN(ba[i]); PIN(bq[i]); PIN(bg[i]); }
#pragma unroll
    for (int i = 0; i < 3; i++) { ha[i] = voffA(3 * lw + i, i, F_ * 2); PIN(ha[i]); }
    lc0 = ((lane & 7) ^ ((((3 * lw) & 1) << 2) + (lane >> 4))) * 8; lc1 = lc0 ^ 32; PIN(lc0); PIN(lc1);
    {
      auto mk = [&](int j, unsigned fbase, int nrows, int ncols) {
        const bool cok = ((lane & 31) >> 1) < ncols;
        pv[j][0] = (cok && (lane >> 5) < nrows) ? fbase + lane * 16 : BIGOFF;
        pv[j][1] = (cok && 2 + (lane >> 5) < nrows) ? fbase + 1024 + lane * 16 : BIGOFF;
      };
      { const int r0 = max(sr - 1, 0), r1 = min(sr + 1, 21); mk(0, WS_FH + r0 * 512, r1 - r0 + 1, 10); }
      { const int r0 = max(32 * aB - 14, 0) >> 7, r1 = (32 * aB + 31) >> 7; mk(1, WS_FQ + r0 * 512, r1 - r0 + 1, 15); }
      { const int r0 = (gr * 128) / 96, r1 = (gr * 128 + 127) / 96; mk(2, WS_FX + r0 * 512, r1 - r0 + 1, 10);
        if (lane >= 32 && lane < (gc == 13 ? 34 : 36)) pv[2][1] = WS_FC + gc * 64 + (lane - 32) * 16; }
      mk(3, WS_FO + sr * 384, 1, sr == 21 ? 4 : 12);
      { const int r0 = (sr * 96) >> 7, r1 = min(sr * 96 + 95, S_ - 1) >> 7; mk(4, WS_FG + r0 * 512, r1 - r0 + 1, 14); }
#pragma unroll
      for (int j = 0; j < 5; j++) { PIN(pv[j][0]); PIN(pv[j][1]); }
      const int s0 = 32 * aB + apar;
#pragma unroll
      for (int i = 0; i < 7; i++) {
        const int e = (7 * lw + i) * 64 + lane, row = e / 40, ch = e - row * 40; const bool isq = row < 16;
        const int grow = isq ? s0 + 2 * row : s0 - 14 + 2 * (row - 16);
        at0[i] = (e < 1560 && grow >= 0) ? (unsigned)(grow * (Q_ * 2) + (isq ? 0 : 1280) + ahh * 640 + ch * 16) : BIGOFF; PIN(at0[i]);
      }
#pragma unroll
      for (int i = 0; i < 4; i++) {
        const int e = (4 * lw + i) * 64 + lane, row = e / 40, ch = e - row * 40; const int grow = s0 - 14 + 2 * row;
        at1[i] = (e < 920 && grow >= 0) ? (unsigned)(grow * (Q_ * 2) + 2560 + ahh * 640 + ch * 16) : BIGOFF; PIN(at1[i]);
      }
      const int R = gr * 128 + 2 * lane, rt = R / 96, rr = R - rt * 96;
      stv = WS_ST + rt * (20 * 768) + rr * 8; PIN(stv);
    }
    auto early_poll = [&](int j) { if (lw == 0) { dma_sc1(ldsbase + L_POLL, pv[j][0], rWS, 0); dma_sc1(ldsbase + L_POLL + 1024, pv[j][1], rWS, 0); } };
    auto dep = [&](int j, bool early) {
      const u64 E = *(volatile LAS u64*)(lds + L_EP);
      const unsigned elo = (unsigned)E, ehi = (unsigned)(E >> 32);
      u32x4 f0 = {0, 0, 0, 0}, f1 = {0, 0, 0, 0};
      if (early) { f0 = *(volatile LAS u32x4*)(lds + L_POLL + lane * 16); f1 = *(volatile LAS u32x4*)(lds + L_POLL + 1024 + lane * 16); }
      for (unsigned spin = 0; spin < SPIN_CAP; spin++) {
        if (spin || early) {
          const bool ok = (pv[j][0] == BIGOFF || (f0.x == elo && f0.y == ehi && f0.z == elo && f0.w == ehi)) &&
                          (pv[j][1] == BIGOFF || (f1.x == elo && f1.y == ehi && f1.z == elo && f1.w == ehi));
          if (__all(ok)) break;
        }
        asm volatile("s_nop 4\n\tbuffer_load_dwordx4 %0, %2, %4, 0 offen sc1\n\tbuffer_load_dwordx4 %1, %3, %4, 0 offen sc1\n\ts_waitcnt vmcnt(0)"
                     : "=&v"(f0), "=&v"(f1) : "v"(pv[j][0]), "v"(pv[j][1]), "s"(rWS) : "memory");
      }
    };
#define STEP(N) do { waitv<N>(); bar(); TRACE(); } while (0)
    const rsrc_t rWQ = mkr(wqkv, D_ * Q_ * 2);
    if (hasS) {
#pragma unroll 1
      for (int c = 2; c < 10; c++) {
        STEP(5); issS(rX, rW1, 10);
        if (c < 5) touch(ldsbase + L_DUM, t50, rWQ, (wq * 3 + c - 2) * 50 * 128);
        if (c == 8 && !hasQ) early_poll(0);
      }
      STEP(5); STEP(0);
    } else {
      for (int i = 0; i < 3; i++) touch(ldsbase + L_DUM, t50, rWQ, (wq * 3 + i) * 50 * 128);
    }
    STAMP();
    const rsrc_t rW2 = mkr(w2, D_ * D_ * 2);
    if (hasQ) {
      kc = qc % 10;
      const unsigned ao = qr * 128 * (D_ * 2), bo = qc * 256;
      auto iss = [&]() {
        dma4s(mAb + slot, ba[0], ba[1], ba[2], ba[3], rX, ao + kc * 128);
        dma4(mBb + slot, bq[0], bq[1], bq[2], bq[3], rWQ, bo + kc * (64 * Q_ * 2));
        kc = (kc + 1 == 10) ? 0 : kc + 1; nexts();
      };
      iss(); iss();
#pragma unroll 1
      for (int c = 2; c < 10; c++) {
        STEP(8); iss();
        if (c == 3) touch(ldsbase + L_DUM, t50, rW2, wq * 50 * 128);
        if (c == 8) { if (hasS) early_poll(0); else early_poll(1); }
      }
      STEP(8); STEP(0);
    } else touch(ldsbase + L_DUM, t50, rW2, wq * 50 * 128);
    STAMP();
    if (hasS) {
      dep(0, true);
      STAMP();
      const rsrc_t rH = mkr(ws + WS_H, S_ * D_ * 2);
      kc = sc;
      auto iss = [&]() {
        const int c0 = kc * 64 + lc0, c1 = kc * 64 + lc1;
        const int h0 = (c0 < 160 ? -(D_ * 2) : (c0 < 320 ? (D_ * 2) : (c0 < 480 ? -2 * (D_ * 2) : 2 * (D_ * 2)))) + sAo;
        const int h1 = (c1 < 160 ? -(D_ * 2) : (c1 < 320 ? (D_ * 2) : (c1 < 480 ? -2 * (D_ * 2) : 2 * (D_ * 2)))) + sAo;
        unsigned v0 = sa[0] + h0; v0 = (int)v0 < 0 ? BIGOFF : v0;
        dma3s(mAs + slot, v0, sa[1] + h1, sa[2] + h0, rH, kc * 128);
        dma2(mBs + slot, sb2[0], sb2[1], rW2, sBo + kc * (64 * D_ * 2));
        kc = (kc + 1 == 10) ? 0 : kc + 1; nexts();
      };
      iss(); iss();
#pragma unroll 1
      for (int c = 2; c < 10; c++) { STEP(5); iss(); if (c == 8) early_poll(1); }
      STEP(5); STEP(0);
    }
    STAMP();
    const rsrc_t rW3 = mkr(w3, D_ * F_ * 2);
    {
      dep(1, true);
      STAMP();
      const rsrc_t rQ = mkr(ws + WS_QKV, S_ * Q_ * 2);
#pragma unroll
      for (int i = 0; i < 7; i++) dma_sc1(ldsbase + slot + (7 * lw + i) * 1024, at0[i], rQ, 0);
      nexts();
#pragma unroll
      for (int i = 0; i < 4; i++) dma_sc1(ldsbase + slot + (4 * lw + i) * 1024, at1[i], rQ, 0);
      nexts();
      STEP(4); STEP(0);
      for (int i = 0; i < 3; i++) touch(ldsbase + L_DUM, t45, rW3, (wq * 3 + i) * 45 * 128);
    }
    STAMP();
    const rsrc_t rWO = mkr(wo, D_ * D_ * 2), rW4 = mkr(w4, F_ * D_ * 2);
    if (hasG) {
      const rsrc_t rX1 = mkr(ws + WS_X1, S_ * D_ * 2);
      kc = gc % 10;
      const int kc1 = (kc + 1 == 10) ? 0 : kc + 1;
      const unsigned ao = gr * 128 * (D_ * 2), bo = gc * 256, slot1 = (slot + SLOT) & (4 * SLOT - 1);
      dma4(mBb + slot, bg[0], bg[1], bg[2], bg[3], rW3, bo + kc * (64 * F_ * 2));
      dma4(mBb + slot1, bg[0], bg[1], bg[2], bg[3], rW3, bo + kc1 * (64 * F_ * 2));
      dep(2, false);
      STAMP();
      dma4s(mAb + slot, ba[0], ba[1], ba[2], ba[3], rX1, ao + kc * 128);
      dma4s(mAb + slot1, ba[0], ba[1], ba[2], ba[3], rX1, ao + kc1 * 128);
#pragma unroll
      for (int i = 0; i < 5; i++) { const int pi = lw + 4 * i; dma_sc1(ldsbase + L_ST + pi * 1024, stv, rWS, pi * 768); }
      if (lw == 0) dma_sc1(ldsbase + L_CS, lane < 32 ? lane * 16 : BIGOFF, rWS, WS_CS + gc * 512);
      kc = (kc1 + 1 == 10) ? 0 : kc1 + 1; nexts(); nexts();
#pragma unroll 1
      for (int c = 2; c < 10; c++) {
        STEP(8);
        dma4s(mAb + slot, ba[0], ba[1], ba[2], ba[3], rX1, ao + kc * 128);
        dma4(mBb + slot, bg[0], bg[1], bg[2], bg[3], rW3, bo + kc * (64 * F_ * 2));
        kc = (kc + 1 == 10) ? 0 : kc + 1; nexts();
        if (c == 3) touch(ldsbase + L_DUM, t50, rWO, wq * 50 * 128);
        if (c == 8) early_poll(3);
      }
      STEP(8); STEP(0);
    } else touch(ldsbase + L_DUM, t50, rWO, wq * 50 * 128);
    STAMP();
    if (hasS) {
      dep(3, hasG);
      STAMP();
      const rsrc_t rO = mkr(ws + WS_O, S_ * D_ * 2);
      kc = sc;
      issS(rO, rWO, 10); issS(rO, rWO, 10);
      for (int i = 0; i < 3; i++) touch(ldsbase + L_DUM, t45, rW4, (wq * 3 + i) * 45 * 128);
#pragma unroll 1
      for (int c = 2; c < 10; c++) { STEP(5); issS(rO, rWO, 10); if (c == 8) early_poll(4); }
      STEP(5); STEP(0);
      STAMP();
      const rsrc_t rG = mkr(ws + WS_H2, S_ * F_ * 2);
      kc = (sc * 5) % 27;
      const unsigned ao = sr * 96 * (F_ * 2);
      dep(4, true);
      STAMP();
      auto iss = [&]() {
        dma3s(mAs + slot, ha[0], ha[1], ha[2], rG, ao + kc * 128);
        dma2(mBs + slot, sb2[0], sb2[1], rW4, sBo + kc * (64 * D_ * 2));
        kc = (kc + 1 == 27) ? 0 : kc + 1; nexts();
      };
      iss(); iss();
#pragma unroll 1
      for (int c = 2; c < 27; c++) { STEP(5); iss(); }
      STEP(5); STEP(0);
    } else {
      for (int i = 0; i < 3; i++) touch(ldsbase + L_DUM, t45, rW4, (wq * 3 + i) * 45 * 128);
    }
    STAMP();
  } else {
    const int wr = w >> 1, wc = w & 1;
    const int m16 = lane & 15, G = lane >> 4;
    const u64 E = __hip_atomic_load((GAS u64*)(ws + WS_EPOCH), __ATOMIC_RELAXED, __HIP_MEMORY_SCOPE_AGENT) + EPOCH_STEP;
    if (w == 3) {
      if (lane < 4) *(LAS unsigned*)(lds + L_ONE + lane * 4) = 0x3F803F80u;
      else if (lane < 8) *(LAS unsigned*)(lds + L_ONE + lane * 4) = 0u;
      if (lane == 0) *(LAS u64*)(lds + L_EP) = E;
      asm volatile("s_waitcnt lgkmcnt(0)" ::: "memory");
    }
    const rsrc_t rWS = mkr(ws, WS_DBG);
    int n = 0;
    unsigned pend = 0;
    auto flush = [&]() {
      if (pend) {
        waitv<0>();
        if (lane == 0) __hip_atomic_store((GAS u64*)(ws + pend) + w, E, __ATOMIC_RELAXED, __HIP_MEMORY_SCOPE_AGENT);
        pend = 0;
      }
    };
    auto publish = [&](unsigned foff) { pend = foff; };
    const int q4 = (lane & 15) >> 2, p4 = lane & 3;
    const int bx = (((q4 >> 1) & 1) << 2) | ((G & 1) << 1);
    const unsigned sA0 = (G ^ ((m16 >> 1) & 7)) * 16, sA1 = ((4 + G) ^ ((m16 >> 1) & 7)) * 16;
    const unsigned brow = BOFF + (8 * G + q4) * 128 + 8 * (p4 & 1);
    const int cst = (G & 1) * 16 + (G >> 1) * 8;

    const unsigned saoff0 = (wr * 48 + m16) * 128 + sA0, saoff1 = (wr * 48 + m16) * 128 + sA1;
    const unsigned sboff0 = brow + (((wc * 4 + 0 + (p4 >> 1)) ^ bx) * 16), sboff1 = brow + (((wc * 4 + 2 + (p4 >> 1)) ^ bx) * 16);
    f32x4 acc[3][2];
    f32x4 aS[3], aQ[3], aC[2];
    u32x2 xt[3][2];
    float gate[3];
    auto small = [&](auto NCHT, auto STATT, int rot) {
      constexpr int NCH = decltype(NCHT)::value; constexpr bool STAT = decltype(STATT)::value;
#pragma unroll
      for (int i = 0; i < 3; i++)
#pragma unroll
        for (int j = 0; j < 2; j++) acc[i][j] = (f32x4){0.f, 0.f, 0.f, 0.f};
      const s16x8 ones = {0x3F80, 0x3F80, 0x3F80, 0x3F80, 0x3F80, 0x3F80, 0x3F80, 0x3F80};
      unsigned spbase = 0, spstep = 0; int kc = rot;
      if (STAT) {
#pragma unroll
        for (int i = 0; i < 3; i++) { aS[i] = (f32x4){0.f, 0.f, 0.f, 0.f}; aQ[i] = (f32x4){0.f, 0.f, 0.f, 0.f}; }
        aC[0] = aC[1] = (f32x4){0.f, 0.f, 0.f, 0.f};
        const int c3 = m16 & 3;
        spbase = c3 == 1 ? L_GW + G * 16 : (c3 == 0 ? L_ONE : L_ONE + 16);
        spstep = c3 == 1 ? 64 : 0;
      }
      auto rd = [&](FragS& Fr) {
        const char* sb = lds + (n & 3) * SLOT;
#pragma unroll
        for (int ks = 0; ks < 2; ks++) {
          s16x4 l0 = __builtin_amdgcn_ds_read_tr16_b64_v4i16((LAS s16x4*)(sb + sboff0 + ks * 4096));
          s16x4 h0 = __builtin_amdgcn_ds_read_tr16_b64_v4i16((LAS s16x4*)(sb + sboff0 + ks * 4096 + 512));
          s16x4 l1 = __builtin_amdgcn_ds_read_tr16_b64_v4i16((LAS s16x4*)(sb + sboff1 + ks * 4096));
          s16x4 h1 = __builtin_amdgcn_ds_read_tr16_b64_v4i16((LAS s16x4*)(sb + sboff1 + ks * 4096 + 512));
          Fr.b[ks][0] = __builtin_shufflevector(l0, h0, 0, 1, 2, 3, 4, 5, 6, 7);
          Fr.b[ks][1] = __builtin_shufflevector(l1, h1, 0, 1, 2, 3, 4, 5, 6, 7);
#pragma unroll
          for (int i = 0; i < 3; i++) Fr.a[ks][i] = *(const s16x8*)(sb + (ks ? saoff1 : saoff0) + i * 2048);
          if (STAT) Fr.sp[ks] = *(const s16x8*)(lds + spbase + spstep * (2 * kc + ks));
        }
        if (STAT) kc = (kc + 1 == NCH) ? 0 : kc + 1;
        n++;
      };
      auto mm = [&](FragS& Fr) {
#pragma unroll
        for (int ks = 0; ks < 2; ks++) {
#pragma unroll
          for (int i = 0; i < 3; i++)
#pragma unroll
            for (int j = 0; j < 2; j++) acc[i][j] = __builtin_amdgcn_mfma_f32_16x16x32_bf16(Fr.b[ks][j], Fr.a[ks][i], acc[i][j], 0, 0, 0);
          if (STAT) {
#pragma unroll
            for (int i = 0; i < 3; i++) {
              aS[i] = __builtin_amdgcn_mfma_f32_16x16x32_bf16(Fr.sp[ks], Fr.a[ks][i], aS[i], 0, 0, 0);
              aQ[i] = __builtin_amdgcn_mfma_f32_16x16x32_bf16(Fr.a[ks][i], Fr.a[ks][i], aQ[i], 0, 0, 0);
            }
#pragma unroll
            for (int j = 0; j < 2; j++) aC[j] = __builtin_amdgcn_mfma_f32_16x16x32_bf16(Fr.b[ks][j], ones, aC[j], 0, 0, 0);
          }
        }
      };
      auto sched = [&]() {
#pragma unroll
        for (int i = 0; i < (STAT ? 8 : 7); i++) { SGB(0x8, 1, 0); SGB(0x100, 2, 0); }
        SGB(0x8, STAT ? 20 : 5, 0);
      };
      FragS F0, F1;
      bar(); SB0;
      if (STAT) {
        const char* sb = lds + (n & 3) * SLOT;
#pragma unroll
        for (int i = 0; i < 3; i++)
#pragma unroll
          for (int j = 0; j < 2; j++)
            xt[i][j] = *(const u32x2*)(sb + (wr * 48 + 16 * i + m16) * 128 + (((wc * 4 + 2 * j + (G >> 1)) ^ ((m16 >> 1) & 7)) * 16) + (G & 1) * 8);
      }
      rd(F0); SB0;
#pragma unroll 1
      for (int c = 1; c + 1 < NCH; c += 2) {
        bar(); LG0; SB0; rd(F1); mm(F0); sched(); SB0;
        bar(); LG0; SB0; rd(F0); mm(F1); sched(); SB0;
        if (c == 3) flush();
      }
      if (NCH % 2 == 0) { bar(); LG0; SB0; rd(F1); mm(F0); sched(); SB0; mm(F1); }
      else mm(F0);
      SB0;
    };
    f32x4 bacc[4][4];
    auto big = [&]() {
#pragma unroll
      for (int i = 0; i < 4; i++)
#pragma unroll
        for (int j = 0; j < 4; j++) bacc[i][j] = (f32x4){0.f, 0.f, 0.f, 0.f};
      const unsigned a0 = (wr * 64 + m16) * 128 + sA0, a1 = (wr * 64 + m16) * 128 + sA1;
      unsigned bo[4];
#pragma unroll
      for (int j = 0; j < 4; j++) bo[j] = brow + wc * 8192 + (((2 * j + (p4 >> 1)) ^ bx) * 16);
      auto rd = [&](FragB& Fr, int ks) {
        const char* sb = lds + (n & 3) * SLOT;
#pragma unroll
        for (int j = 0; j < 4; j++) {
          s16x4 lo = __builtin_amdgcn_ds_read_tr16_b64_v4i16((LAS s16x4*)(sb + bo[j] + ks * 4096));
          s16x4 hi = __builtin_amdgcn_ds_read_tr16_b64_v4i16((LAS s16x4*)(sb + bo[j] + ks * 4096 + 512));
          Fr.b[j] = __builtin_shufflevector(lo, hi, 0, 1, 2, 3, 4, 5, 6, 7);
        }
#pragma unroll
        for (int i = 0; i < 4; i++) Fr.a[i] = *(const s16x8*)(sb + (ks ? a1 : a0) + i * 2048);
      };
      auto mm = [&](FragB& Fr) {
#pragma unroll
        for (int i = 0; i < 4; i++)
#pragma unroll
          for (int j = 0; j < 4; j++) bacc[i][j] = __builtin_amdgcn_mfma_f32_16x16x32_bf16(Fr.b[j], Fr.a[i], bacc[i][j], 0, 0, 0);
      };
      auto sched = [&]() {
#pragma unroll
        for (int i = 0; i < 12; i++) { SGB(0x8, 1, 0); SGB(0x100, 1, 0); }
        SGB(0x8, 4, 0);
      };
      FragB F0, F1;
      bar(); SB0;
      rd(F0, 0); SB0;
#pragma unroll 1
      for (int c = 0; c < 9; c++) {
        rd(F1, 1); mm(F0); sched(); SB0;
        bar(); n++; SB0;
        rd(F0, 0); mm(F1); sched(); SB0;
        if (c == 1) flush();
      }
      rd(F1, 1); mm(F0); sched(); SB0;
      mm(F1); n++; SB0;
    };
    using I10 = std::integral_constant<int, 10>; using I27 = std::integral_constant<int, 27>;
    using TT = std::integral_constant<bool, true>; using TF = std::integral_constant<bool, false>;
    const int rot10 = sc;
    const unsigned srow = sr * 96 + wr * 48 + m16;
    const unsigned scolb = (sc * 64 + wc * 32 + cst) * 2;

    if (hasS) {
      small(I10{}, TT{}, rot10);
      STAMP();
      const rsrc_t rH = mkr(ws + WS_H, S_ * D_ * 2);
#pragma unroll
      for (int i = 0; i < 3; i++) {
        const float mean = aS[i][0] * (1.0f / D_);
        const int r = m16 & 3;
        float q = r == 0 ? aQ[i][0] : (r == 1 ? aQ[i][1] : (r == 2 ? aQ[i][2] : aQ[i][3]));
        q = __shfl(q, m16 + 16 * (m16 >> 2));
        const float var = fmaxf(q * (1.0f / D_) - mean * mean, 0.f);
        const float rstd = __builtin_amdgcn_rsqf(var + 1e-5f);
        gate[i] = __builtin_amdgcn_rcpf(1.0f + __builtin_amdgcn_exp2f(-1.4426950408889634f * aS[i][1]));
        unsigned pk[2][2];
#pragma unroll
        for (int j = 0; j < 2; j++) {
          float v[4];
#pragma unroll
          for (int e = 0; e < 4; e++) v[e] = gelu_t((acc[i][j][e] - mean * aC[j][e]) * rstd);
          pk[j][0] = cvt_pk(v[0], v[1]); pk[j][1] = cvt_pk(v[2], v[3]);
        }
        __builtin_amdgcn_raw_buffer_store_b128(pack2(pk[0][0], pk[0][1], pk[1][0], pk[1][1]), rH, (srow + 16 * i) * (D_ * 2) + scolb, 0, 16);
      }
      publish(WS_FH + (sr * 16 + sc) * 32);
      if (!hasQ) flush();
    }
    STAMP();
    if (hasQ) {
      big();
      STAMP();
      const rsrc_t rQ = mkr(ws + WS_QKV, S_ * Q_ * 2);
#pragma unroll
      for (int i = 0; i < 4; i++)
#pragma unroll
        for (int jp = 0; jp < 2; jp++) {
          const f32x4 u = bacc[i][2 * jp], v = bacc[i][2 * jp + 1];
          __builtin_amdgcn_raw_buffer_store_b128(pack2(cvt_pk(u[0], u[1]), cvt_pk(u[2], u[3]), cvt_pk(v[0], v[1]), cvt_pk(v[2], v[3])), rQ,
                                                 (qr * 128 + wr * 64 + 16 * i + m16) * (Q_ * 2) + (qc * 128 + wc * 64 + jp * 32 + cst) * 2, 0, 16);
        }
      publish(WS_FQ + (qr * 16 + qc) * 32);
      if (!hasS) flush();
    }
    STAMP();
    if (hasS) {
      small(I10{}, TF{}, rot10);
      STAMP();
      const rsrc_t rX1 = mkr(ws + WS_X1, S_ * D_ * 2);
      float ssum = 0.f, ssq = 0.f;
#pragma unroll
      for (int i = 0; i < 3; i++) {
        float s = 0.f, q = 0.f;
#pragma unroll
        for (int j = 0; j < 2; j++) {
          const unsigned p0 = cvt_pk(bflo(xt[i][j].x) + acc[i][j][0], bfhi(xt[i][j].x) + acc[i][j][1]);
          const unsigned p1 = cvt_pk(bflo(xt[i][j].y) + acc[i][j][2], bfhi(xt[i][j].y) + acc[i][j][3]);
          xt[i][j] = (u32x2){p0, p1};
          s = dot2(p0, 0x3F803F80u, s); s = dot2(p1, 0x3F803F80u, s);
          q = dot2(p0, p0, q); q = dot2(p1, p1, q);
        }
        __builtin_amdgcn_raw_buffer_store_b128(pack2(xt[i][0].x, xt[i][0].y, xt[i][1].x, xt[i][1].y), rX1, (srow + 16 * i) * (D_ * 2) + scolb, 0, 16);
        s += __shfl_xor(s, 16); s += __shfl_xor(s, 32);
        q += __shfl_xor(q, 16); q += __shfl_xor(q, 32);
        if (G == i) { ssum = s; ssq = q; }
      }
      if (G < 3) {
        u32x2 sv = {__float_as_uint(ssum), __float_as_uint(ssq)};
        __builtin_amdgcn_raw_buffer_store_b64(sv, rWS, WS_ST + ((sr * 20 + sc * 2 + wc) * 96 + wr * 48 + 16 * G + m16) * 8, 0, 16);
      }
      publish(WS_FX + (sr * 16 + sc) * 32);
    }
    STAMP();
    {
      const int hl = lane & 15, ri = w * 4 + (lane >> 4);
      char* s0p = lds + (n & 3) * SLOT;
      const char* s1p = lds + ((n + 1) & 3) * SLOT;
      bar(); SB0;
      unsigned qv[10];
      {
        const char* qp = s0p + ri * 640 + hl * 40;
#pragma unroll
        for (int d = 0; d < 5; d++) { const u32x2 t = *(const u32x2*)(qp + d * 8); qv[2 * d] = t.x; qv[2 * d + 1] = t.y; }
      }
      float lg[8];
      const int srow_g = 32 * aB + apar + 2 * ri;
#pragma unroll
      for (int ww = 0; ww < 8; ww++) {
        const char* kp = s0p + (16 + ri + 7 - ww) * 640 + hl * 40;
        float a0 = 0.f, a1 = 0.f;
#pragma unroll
        for (int d = 0; d < 5; d++) { const u32x2 t = *(const u32x2*)(kp + d * 8); a0 = dot2(qv[2 * d], t.x, a0); a1 = dot2(qv[2 * d + 1], t.y, a1); }
        lg[ww] = (srow_g - 2 * ww >= 0) ? (a0 + a1) * (0.22360679774997896f * 1.4426950408889634f) : -1e30f;
      }
      float mx = lg[0];
#pragma unroll
      for (int ww = 1; ww < 8; ww++) mx = fmaxf(mx, lg[ww]);
      float sum = 0.f;
#pragma unroll
      for (int ww = 0; ww < 8; ww++) { lg[ww] = __builtin_amdgcn_exp2f(lg[ww] - mx); sum += lg[ww]; }
      const float inv = __builtin_amdgcn_rcpf(sum);
      unsigned pp[4];
#pragma unroll
      for (int k = 0; k < 4; k++) pp[k] = cvt_pk(lg[2 * k] * inv, lg[2 * k + 1] * inv);
      SB0; flush(); bar(); SB0;
      float o[20];
#pragma unroll
      for (int d = 0; d < 20; d++) o[d] = 0.f;
#pragma unroll
      for (int k = 0; k < 4; k++) {
        const char* va = s1p + (ri + 7 - 2 * k) * 640 + hl * 40;
        const char* vb = va - 640;
#pragma unroll
        for (int d = 0; d < 5; d++) {
          const u32x2 ta = *(const u32x2*)(va + d * 8), tb = *(const u32x2*)(vb + d * 8);
          o[4 * d + 0] = dot2(__builtin_amdgcn_perm(tb.x, ta.x, 0x05040100u), pp[k], o[4 * d + 0]);
          o[4 * d + 1] = dot2(__builtin_amdgcn_perm(tb.x, ta.x, 0x07060302u), pp[k], o[4 * d + 1]);
          o[4 * d + 2] = dot2(__builtin_amdgcn_perm(tb.y, ta.y, 0x05040100u), pp[k], o[4 * d + 2]);
          o[4 * d + 3] = dot2(__builtin_amdgcn_perm(tb.y, ta.y, 0x07060302u), pp[k], o[4 * d + 3]);
        }
      }
      {
        char* op = s0p + ri * 640 + hl * 40;
#pragma unroll
        for (int d = 0; d < 5; d++) *(u32x2*)(op + d * 8) = (u32x2){cvt_pk(o[4 * d], o[4 * d + 1]), cvt_pk(o[4 * d + 2], o[4 * d + 3])};
      }
      asm volatile("s_waitcnt lgkmcnt(0)" ::: "memory");
      const rsrc_t rO = mkr(ws + WS_O, S_ * D_ * 2);
#pragma unroll
      for (int t = 0; t < 3; t++) {
        const int e = t * 64 + lane, rw = e / 40, ch = e - rw * 40;
        if (e < 160) {
          const u32x4 v = *(const u32x4*)(s0p + w * 2560 + e * 16);
          __builtin_amdgcn_raw_buffer_store_b128(v, rO, (32 * aB + apar + 2 * (w * 4 + rw)) * (D_ * 2) + ahh * 640 + ch * 16, 0, 16);
        }
      }
      publish(WS_FO + (aB * 4 + (li & 3)) * 32);
      flush();
      n += 2;
    }
    STAMP();
    if (hasG) {
      big();
      STAMP();
      float s = 0.f, q = 0.f;
#pragma unroll
      for (int pi = 0; pi < 20; pi++) { const f32x2 t = *(const f32x2*)(lds + L_ST + pi * 1024 + (wr * 64 + lane) * 8); s += t.x; q += t.y; }
      const float mean_l = s * (1.0f / D_);
      const float rstd_l = __builtin_amdgcn_rsqf(fmaxf(q * (1.0f / D_) - mean_l * mean_l, 0.f) + 1e-5f);
      f32x4 cs[4];
#pragma unroll
      for (int j = 0; j < 4; j++) cs[j] = *(const f32x4*)(lds + L_CS + (wc * 64 + 16 * j + 4 * G) * 4);
      const rsrc_t rG = mkr(ws + WS_H2, S_ * F_ * 2);
      const bool colok = gc * 128 + wc * 64 < F_;
#pragma unroll
      for (int i = 0; i < 4; i++) {
        const float mean = __shfl(mean_l, 16 * i + m16), rstd = __shfl(rstd_l, 16 * i + m16);
        unsigned pk[4][2];
#pragma unroll
        for (int j = 0; j < 4; j++) {
          float v[4];
#pragma unroll
          for (int e = 0; e < 4; e++) v[e] = gelu_t((bacc[i][j][e] - mean * cs[j][e]) * rstd);
          pk[j][0] = cvt_pk(v[0], v[1]); pk[j][1] = cvt_pk(v[2], v[3]);
        }
        if (colok) {
#pragma unroll
          for (int jp = 0; jp < 2; jp++)
            __builtin_amdgcn_raw_buffer_store_b128(pack2(pk[2 * jp][0], pk[2 * jp][1], pk[2 * jp + 1][0], pk[2 * jp + 1][1]), rG,
                                                   (gr * 128 + wr * 64 + 16 * i + m16) * (F_ * 2) + (gc * 128 + wc * 64 + jp * 32 + cst) * 2, 0, 16);
        }
      }
      publish(WS_FG + (gr * 16 + gc) * 32);
    }
    STAMP();
    if (hasS) {
      small(I10{}, TF{}, rot10);
      STAMP();
      f32x4 tacc[3][2];
#pragma unroll
      for (int i = 0; i < 3; i++)
#pragma unroll
        for (int j = 0; j < 2; j++) {
          const float g = gate[i], h = 1.0f - g;
          tacc[i][j] = (f32x4){g * acc[i][j][0] + h * bflo(xt[i][j].x), g * acc[i][j][1] + h * bfhi(xt[i][j].x),
                               g * acc[i][j][2] + h * bflo(xt[i][j].y), g * acc[i][j][3] + h * bfhi(xt[i][j].y)};
        }
      small(I27{}, TF{}, 0);
      STAMP();
      const rsrc_t rOut = mkr(out, S_ * D_ * 2);
#pragma unroll
      for (int i = 0; i < 3; i++) {
        const float h = 1.0f - gate[i];
        unsigned pk[2][2];
#pragma unroll
        for (int j = 0; j < 2; j++) {
          pk[j][0] = cvt_pk(tacc[i][j][0] + h * acc[i][j][0], tacc[i][j][1] + h * acc[i][j][1]);
          pk[j][1] = cvt_pk(tacc[i][j][2] + h * acc[i][j][2], tacc[i][j][3] + h * acc[i][j][3]);
        }
        __builtin_amdgcn_raw_buffer_store_b128(pack2(pk[0][0], pk[0][1], pk[1][0], pk[1][1]), rOut, (srow + 16 * i) * (D_ * 2) + scolb, 0, 0);
      }
    }
    flush();
    if (b == 255 && w == 0) {
      const unsigned elo = (unsigned)E, ehi = (unsigned)(E >> 32);
      for (unsigned spin = 0; spin < SPIN_CAP; spin++) {
        bool ok = true;
#pragma unroll
        for (int t = 0; t < 8; t++) {
          const u32x4 f = __builtin_amdgcn_raw_buffer_load_b128(rWS, WS_FO + (t * 64 + lane) * 16, 0, 16);
          ok = ok && f.x == elo && f.y == ehi && f.z == elo && f.w == ehi;
        }
        if (__all(ok)) break;
        __builtin_amdgcn_s_sleep(4);
      }
      if (lane == 0) __hip_atomic_store((GAS u64*)(ws + WS_EPOCH), E, __ATOMIC_RELAXED, __HIP_MEMORY_SCOPE_AGENT);
    }
    STAMP();
  }
}

extern "C" void kernel_launch(void* const* d_in, const int* in_sizes, int n_in, void* d_out, int out_size, void* d_ws, size_t ws_size,
                              hipStream_t stream) {
  (void)in_sizes; (void)n_in; (void)out_size; (void)ws_size;
  (void)hipFuncSetAttribute((const void*)fused_kernel, hipFuncAttributeMaxDynamicSharedMemorySize, LDS_BYTES);
  fused_kernel<<<256, 512, LDS_BYTES, stream>>>((const u16*)d_in[0], (const u16*)d_in[1], (const u16*)d_in[2], (const u16*)d_in[4],
                                                (const u16*)d_in[5], (const u16*)d_in[6], (const u16*)d_in[7], (const u16*)d_in[8],
                                                (u16*)d_out, (char*)d_ws);
}
```

```cpp
#include <hip/hip_runtime.h>
#include <hip/hip_bf16.h>

typedef unsigned short u16;
typedef unsigned long long u64;
typedef unsigned u32x4 __attribute__((ext_vector_type(4)));
typedef unsigned u32x2 __attribute__((ext_vector_type(2)));
typedef short s16x4 __attribute__((ext_vector_type(4)));
typedef short s16x8 __attribute__((ext_vector_type(8)));
typedef float f32x4 __attribute__((ext_vector_type(4)));
typedef float f32x2 __attribute__((ext_vector_type(2)));
typedef __bf16 bf16x2_t __attribute__((ext_vector_type(2)));
#define LAS __attribute__((address_space(3)))
#define GAS __attribute__((address_space(1)))
typedef __amdgpu_buffer_rsrc_t rsrc_t;

#define DBG_STAMPS 0
#define FS 256

constexpr int S_ = 2048, D_ = 640, F_ = 1728, Q_ = 1920;
constexpr unsigned WS_EPOCH = 0;
constexpr unsigned FSZ = 1024 * FS;
constexpr unsigned WS_FH   = 4096;
constexpr unsigned WS_FQ   = WS_FH + FSZ;
constexpr unsigned WS_FX   = WS_FQ + FSZ;
constexpr unsigned WS_FO   = WS_FX + FSZ;
constexpr unsigned WS_FG   = WS_FO + FSZ;
constexpr unsigned WS_FC   = WS_FG + FSZ;
constexpr unsigned WS_H    = WS_FC + FSZ;
constexpr unsigned WS_QKV  = WS_H + S_ * D_ * 2;
constexpr unsigned WS_X1   = WS_QKV + S_ * Q_ * 2;
constexpr unsigned WS_O    = WS_X1 + S_ * D_ * 2;
constexpr unsigned WS_H2   = WS_O + S_ * D_ * 2;
constexpr unsigned WS_ST   = WS_H2 + S_ * F_ * 2;
constexpr unsigned ST_LD = 2112 * 8;
constexpr unsigned WS_CS   = WS_ST + 20 * ST_LD;
constexpr unsigned WS_DBG  = WS_CS + 32768;
constexpr unsigned SLOT = 32768, BOFF = 16384;
constexpr unsigned L_ST = 131072, L_CS = L_ST + 20480, L_RC = L_CS + 4096, L_CF = L_RC + 2048, L_GW = L_CF + 512, L_ONE = L_GW + 2048;
constexpr unsigned L_POLL = L_ONE + 64, L_DUM = L_POLL + 2048;
constexpr unsigned LDS_BYTES = L_DUM + 256;
constexpr u64 EPOCH_STEP = 0x9E3779B97F4A7C15ull;
constexpr unsigned BIGOFF = 0x7fffff00u;
constexpr unsigned SPIN_CAP = 1u << 17;

template <int N> __device__ __forceinline__ void waitv() { asm volatile("s_waitcnt vmcnt(%0)" ::"i"(N) : "memory"); }
__device__ __forceinline__ void bar() { asm volatile("" ::: "memory"); __builtin_amdgcn_s_barrier(); asm volatile("" ::: "memory"); }
__device__ __forceinline__ unsigned cvt_pk(float lo, float hi) { f32x2 f = {lo, hi}; bf16x2_t b = __builtin_convertvector(f, bf16x2_t); return __builtin_bit_cast(unsigned, b); }
__device__ __forceinline__ float bflo(unsigned v) { return __uint_as_float(v << 16); }
__device__ __forceinline__ float bfhi(unsigned v) { return __uint_as_float(v & 0xffff0000u); }
__device__ __forceinline__ float dot2(unsigned a, unsigned b, float c) { return __builtin_amdgcn_fdot2_f32_bf16(__builtin_bit_cast(bf16x2_t, a), __builtin_bit_cast(bf16x2_t, b), c, false); }
__device__ __forceinline__ rsrc_t mkr(const void* p, unsigned bytes) { return __builtin_amdgcn_make_buffer_rsrc((void*)p, (short)0, (int)bytes, 0x00020000); }
__device__ __forceinline__ int vrow(int v) { return (v & ~31) + 2 * (v & 15) + ((v >> 4) & 1); }
__device__ __forceinline__ int rowv(int r) { return (r & ~31) + ((r & 1) << 4) + ((r & 31) >> 1); }
__device__ __forceinline__ float sum16(float a) { auto r = __builtin_amdgcn_permlane16_swap(__float_as_uint(a), __float_as_uint(a), false, false); return __uint_as_float(r[0]) + __uint_as_float(r[1]); }
__device__ __forceinline__ float sum32(float a) { auto r = __builtin_amdgcn_permlane32_swap(__float_as_uint(a), __float_as_uint(a), false, false); return __uint_as_float(r[0]) + __uint_as_float(r[1]); }
__device__ __forceinline__ void dma(unsigned m0v, unsigned vo, rsrc_t r, unsigned so) {
  asm volatile("s_mov_b32 m0, %0\n\ts_nop 3\n\tbuffer_load_dwordx4 %1, %2, %3 offen lds" ::"s"(m0v), "v"(vo), "s"(r), "s"(so) : "memory");
}
__device__ __forceinline__ void dma_sc1(unsigned m0v, unsigned vo, rsrc_t r, unsigned so) {
  asm volatile("s_mov_b32 m0, %0\n\ts_nop 3\n\tbuffer_load_dwordx4 %1, %2, %3 offen sc1 lds" ::"s"(m0v), "v"(vo), "s"(r), "s"(so) : "memory");
}
constexpr float GK1 = -2.0f * 0.7978845608028654f * 1.4426950408889634f, GK2 = GK1 * 0.044715f;
__device__ __forceinline__ unsigned gelu_pk(f32x2 z, float ga, float gb, float gs) {
  const f32x2 t = z * (z * z * gb + ga);
  f32x2 e = {__builtin_amdgcn_exp2f(t.x), __builtin_amdgcn_exp2f(t.y)};
  e = e * gs + gs;
  const f32x2 r = {__builtin_amdgcn_rcpf(e.x), __builtin_amdgcn_rcpf(e.y)};
  const f32x2 o = z * r;
  return __builtin_bit_cast(unsigned, __builtin_convertvector(o, bf16x2_t));
}
__device__ __forceinline__ void dma2(unsigned m0v, unsigned v0, unsigned v1, rsrc_t r, unsigned so) {
  asm volatile("s_mov_b32 m0, %0\n\ts_nop 3\n\tbuffer_load_dwordx4 %1, %3, %4 offen lds\n\tbuffer_load_dwordx4 %2, %3, %4 offen offset:1024 lds"
               ::"s"(m0v), "v"(v0), "v"(v1), "s"(r), "s"(so) : "memory");
}
__device__ __forceinline__ void dma2s(unsigned m0v, unsigned v0, unsigned v1, rsrc_t r, unsigned so) {
  asm volatile("s_mov_b32 m0, %0\n\ts_nop 3\n\tbuffer_load_dwordx4 %1, %3, %4 offen sc1 lds\n\tbuffer_load_dwordx4 %2, %3, %4 offen offset:1024 sc1 lds"
               ::"s"(m0v), "v"(v0), "v"(v1), "s"(r), "s"(so) : "memory");
}
__device__ __forceinline__ void dma4(unsigned m0v, unsigned v0, unsigned v1, unsigned v2, unsigned v3, rsrc_t r, unsigned so) {
  asm volatile("s_mov_b32 m0, %0\n\ts_nop 3\n\tbuffer_load_dwordx4 %1, %5, %6 offen lds\n\tbuffer_load_dwordx4 %2, %5, %6 offen offset:1024 lds\n\t"
               "buffer_load_dwordx4 %3, %5, %6 offen offset:2048 lds\n\tbuffer_load_dwordx4 %4, %5, %6 offen offset:3072 lds"
               ::"s"(m0v), "v"(v0), "v"(v1), "v"(v2), "v"(v3), "s"(r), "s"(so) : "memory");
}
__device__ __forceinline__ void dma3s(unsigned m0v, unsigned v0, unsigned v1, unsigned v2, rsrc_t r, unsigned so) {
  asm volatile("s_mov_b32 m0, %0\n\ts_nop 3\n\tbuffer_load_dwordx4 %1, %4, %5 offen sc1 lds\n\tbuffer_load_dwordx4 %2, %4, %5 offen offset:1024 sc1 lds\n\t"
               "buffer_load_dwordx4 %3, %4, %5 offen offset:2048 sc1 lds"
               ::"s"(m0v), "v"(v0), "v"(v1), "v"(v2), "s"(r), "s"(so) : "memory");
}
__device__ __forceinline__ void dma4s(unsigned m0v, unsigned v0, unsigned v1, unsigned v2, unsigned v3, rsrc_t r, unsigned so) {
  asm volatile("s_mov_b32 m0, %0\n\ts_nop 3\n\tbuffer_load_dwordx4 %1, %5, %6 offen sc1 lds\n\tbuffer_load_dwordx4 %2, %5, %6 offen offset:1024 sc1 lds\n\t"
               "buffer_load_dwordx4 %3, %5, %6 offen offset:2048 sc1 lds\n\tbuffer_load_dwordx4 %4, %5, %6 offen offset:3072 sc1 lds"
               ::"s"(m0v), "v"(v0), "v"(v1), "v"(v2), "v"(v3), "s"(r), "s"(so) : "memory");
}
__device__ __forceinline__ void touch(unsigned m0v, unsigned vo, rsrc_t r, unsigned so) {
  asm volatile("s_mov_b32 m0, %0\n\ts_nop 3\n\tbuffer_load_dword %1, %2, %3 offen lds" ::"s"(m0v), "v"(vo), "s"(r), "s"(so) : "memory");
}
__device__ __forceinline__ u32x4 pack2(unsigned a0, unsigned a1, unsigned b0, unsigned b1) {
  auto r0 = __builtin_amdgcn_permlane16_swap(a0, b0, false, false);
  auto r1 = __builtin_amdgcn_permlane16_swap(a1, b1, false, false);
  return (u32x4){r0[0], r1[0], r0[1], r1[1]};
}

struct FragS { s16x8 a[2][3]; s16x8 b[2][2]; s16x8 sp[2]; };
struct FragB { s16x8 a[4]; s16x8 b[4]; };
#define SGB __builtin_amdgcn_sched_group_barrier
#define SB0 __builtin_amdgcn_sched_barrier(0)
#define LG0 __builtin_amdgcn_s_waitcnt(0xC07F)
#define CPRIO 3
#define PSLEEP 1
#define ROTG 0
#define TRICKLE 0
#define CTOUCH 15
#define KNAME "_Z12fused_kernelPKtS0_S0_S0_S0_S0_S0_S0_PtPc"
#define WARM 3
#define CSLATE 0
template <int N> using IC = std::integral_constant<int, N>;

__global__ __launch_bounds__(512, 2) void fused_kernel(const u16* __restrict__ x, const u16* __restrict__ wqkv, const u16* __restrict__ wo,
                                                       const u16* __restrict__ w1, const u16* __restrict__ w2, const u16* __restrict__ w3,
                                                       const u16* __restrict__ w4, const u16* __restrict__ gw, u16* __restrict__ out, char* __restrict__ ws) {
  extern __shared__ __attribute__((aligned(16))) char lds[];
  const int tid = threadIdx.x, lane = tid & 63, w = __builtin_amdgcn_readfirstlane(tid >> 6);
  const int b = blockIdx.x, xg = b & 7, li = b >> 3;
  const int sstart = (55 * xg + 1) >> 1, scnt = ((55 * (xg + 1) + 1) >> 1) - sstart;
  const bool hasS = li < scnt;
  const int sj = sstart + li, sr = sj / 10, sc = sj - sr * 10;
  const unsigned ldsbase = (unsigned)(size_t)(LAS char*)lds;
#define STAMP() do {} while (0)
  const int lw = w & 3, l3 = lane >> 3;
  auto voffX = [&](int p, int i) { return vrow(8 * p + l3) * (D_ * 2) + (((lane & 7) ^ (((p & 1) << 2) + (lane >> 4))) * 16) + (4 - i) * 1024; };
  auto bswz = [&](int pp, int i) { return (pp >> 3) * 128 + (((lane & 7) ^ ((((l3 >> 1) & 1) << 2) | ((pp & 1) << 1))) * 16) - i * 1024; };
  auto voffB = [&](int pp, int i, unsigned ld) { return (8 * (pp & 7) + l3) * ld + bswz(pp, i); };
  unsigned sa[3], sb2[2];
#pragma unroll
  for (int i = 0; i < 3; i++) sa[i] = voffX(3 * lw + i, i);
#pragma unroll
  for (int i = 0; i < 2; i++) sb2[i] = voffB(2 * lw + i, i, D_ * 2);
  const unsigned mAs = ldsbase + 3 * lw * 1024, mBs = ldsbase + BOFF + 2 * lw * 1024;
  const rsrc_t rX = mkr((const char*)x - 4096, S_ * D_ * 2 + 4096), rW1 = mkr(w1, D_ * D_ * 2);
  const unsigned sAo = sr * 96 * (D_ * 2), sBo = sc * 128;
  if (hasS) {
    const int k0 = w < 4 ? (sc == 9 ? 0 : sc + 1) : sc; const unsigned s0 = w < 4 ? SLOT : 0;
    dma3s(mAs + s0, sa[0], sa[1], sa[2], rX, sAo + k0 * 128);
    dma2(mBs + s0, sb2[0], sb2[1], rW1, sBo + k0 * (64 * D_ * 2));
  }
  auto loadE = [&]() {
    u64 e;
    asm volatile("s_load_dwordx2 %0, %1, 0x0\n\ts_waitcnt lgkmcnt(0)" : "=s"(e) : "s"(ws) : "memory");
    return e + EPOCH_STEP;
  };
  const bool hasQ = li >= 2;  const int qi = li - 2, qr = 2 * xg + qi / 15, qc = qi % 15;
  const bool hasG = li >= 4;  const int gi = li - 4, gr = 2 * xg + gi / 14, gc = gi % 14;
  const int aT = 16 * xg + (li >> 1), ahh = li & 1;
  const rsrc_t rWS = mkr(ws, WS_DBG);

  if (w >= 4) {
    unsigned slot = 0;
#define TRACE() do {} while (0)
#define WAITV(N) waitv<N>()
#define PIN(v) asm volatile("" : "+v"(v))
#define STEP(N) do { WAITV(N); bar(); TRACE(); } while (0)
#define STEP2(c, N1, N0) do { if (c) WAITV(N1); else WAITV(N0); bar(); TRACE(); } while (0)
    auto nx = [&](unsigned s) { return (s + SLOT) & (4 * SLOT - 1); };
    const unsigned mAb = ldsbase + 4 * lw * 1024, mBb = ldsbase + BOFF + 4 * lw * 1024;
    int kc;
    auto iss1 = [&]() {
      dma3s(mAs + slot, sa[0], sa[1], sa[2], rX, sAo + kc * 128);
      dma2(mBs + slot, sb2[0], sb2[1], rW1, sBo + kc * (64 * D_ * 2));
      kc = (kc + 1 == 10) ? 0 : kc + 1; slot = nx(slot);
    };
    if (hasS) {
      if (lw < 2) dma(ldsbase + L_GW + lw * 1024, lane * 16, mkr(gw, D_ * 2), lw * 1024);
      kc = sc >= 8 ? sc - 8 : sc + 2; slot = 2 * SLOT;
      iss1();
    }
    u64 pc_; unsigned cl1_, cl2_, pco_;
    asm volatile("s_getpc_b64 %0\n.Lpc_here:\n\ts_mov_b32 %1, .Lend_ld-" KNAME "\n\ts_mov_b32 %2, .Lend_cp-" KNAME "\n\ts_mov_b32 %3, .Lpc_here-" KNAME
                 : "=s"(pc_), "=s"(cl1_), "=s"(cl2_), "=s"(pco_));
    const rsrc_t rC = mkr((const char*)(pc_ - pco_), max(cl1_, cl2_));
    const unsigned tcode = lane < 4 ? (unsigned)((li * 4 + lw) * 4 + lane) * 128u : BIGOFF;
#define CODE_TOUCH(k) do { if (CTOUCH & (k)) touch(ldsbase + L_DUM, tcode, rC, 0); } while (0)
    CODE_TOUCH(1);
    STAMP();
    unsigned ba[4], bq[4];
#pragma unroll
    for (int i = 0; i < 4; i++) { ba[i] = voffX(4 * lw + i, i); bq[i] = voffB(4 * lw + i, i, Q_ * 2); PIN(ba[i]); PIN(bq[i]); }
    const rsrc_t rWQ = mkr(wqkv, D_ * Q_ * 2);
    const unsigned qao = qr * 128 * (D_ * 2), qbo = qc * 256;
    int kq = (qc + ROTG * (qr & 1)) % 10;
    auto issQ = [&]() {
      dma4s(mAb + slot, ba[0], ba[1], ba[2], ba[3], rX, qao + kq * 128);
      dma4(mBb + slot, bq[0], bq[1], bq[2], bq[3], rWQ, qbo + kq * (64 * Q_ * 2));
      kq = (kq + 1 == 10) ? 0 : kq + 1; slot = nx(slot);
    };
    const unsigned pid = (xg * 4 + li - 28) * 4 + lw, t50 = lane < 50 ? lane * 128 : BIGOFF, t45 = lane < 45 ? lane * 128 : BIGOFF;
    if (!hasS) {
      if (li >= 28 && (WARM & 1)) {
        for (int i = 0; i < 3; i++) touch(ldsbase + L_DUM, t50, rWQ, (i * 128 + pid) * 50 * 128);
        touch(ldsbase + L_DUM, t50, mkr(w2, D_ * D_ * 2), pid * 50 * 128);
      }
      slot = 2 * SLOT; issQ(); issQ(); issQ();
    }
    unsigned fa[3], fh[3], fb[4], sp2[2], bg[4], oa[2], ob[2], pv[6], l16 = lane * 16, kv0, kv1;
    auto voffP = [&](int pp, int i, unsigned ld) { const int ii = pp & 7;
      return (32 * (ii >> 2) + 16 * (l3 >> 2) + 4 * (ii & 3) + (l3 & 3)) * ld + bswz(pp, i); };
#pragma unroll
    for (int i = 0; i < 3; i++) { const int p = 3 * lw + i;
      fa[i] = l16 + ((p >> 1) * 20 + (p & 1) - i) * 1024; fh[i] = l16 + ((p >> 1) * 54 + (p & 1) - i) * 1024; PIN(fa[i]); PIN(fh[i]); }
#pragma unroll
    for (int i = 0; i < 4; i++) { const int p = 4 * lw + i;
      fb[i] = l16 + ((p >> 1) * 20 + (p & 1) - i) * 1024; bg[i] = voffP(p, i, F_ * 2); PIN(fb[i]); PIN(bg[i]); }
#pragma unroll
    for (int i = 0; i < 2; i++) { const int p = 2 * lw + i;
      sp2[i] = voffP(p, i, D_ * 2);
      oa[i] = (p * 20 + 4 * (p & 1) + l3) * (D_ * 2) + bswz(p, i);
      ob[i] = ((2 * p + (l3 >> 2)) * 20 + ((l3 >> 2) ? 0 : 16) + (l3 & 3)) * (D_ * 2) + bswz(p, i);
      PIN(sp2[i]); PIN(oa[i]); PIN(ob[i]); }
    {
      const int m = lane & 15, cur = (lane >> 4) & 1;
      kv1 = (cur || (aT >= 2 && m >= 8)) ? (unsigned)((cur ? aT : aT - 2) * (60 * 1024) + ((lane >> 5) * 16 + m) * 16) : BIGOFF;
      kv0 = kv1 + 512; PIN(kv0); PIN(kv1); PIN(l16);
      { const int cg = lane - 40; const bool pos = cg < 5 || (cg >= 10 && cg < 15);
        const int idx = lane < 40 ? 40 * sr + lane : (pos ? (2 * sr - 1) * 20 + cg : (2 * sr + 2) * 20 + cg);
        const bool val = lane < 40 || (lane < 60 && (pos ? sr > 0 : sr < 21));
        pv[0] = val ? WS_FH + idx * FS : BIGOFF; }
      { const int e = lane < 15 ? lane : lane - 10, part = e / 5, rr = lane < 15 ? (aT >> 2) : ((aT - 2) >> 2);
        const bool val = lane < 15 || (lane < 25 && aT >= 2);
        pv[1] = val ? WS_FQ + (rr * 30 + part * 10 + 5 * ahh + (e - part * 5)) * FS : BIGOFF; }
      { const int r0 = (gr * 128) / 48, nf = ((gr * 128 + 127) / 48 - r0 + 1) * 20;
        pv[2] = lane < nf ? WS_FX + (r0 * 20 + lane) * FS : BIGOFF;
        pv[5] = lane < 16 ? (64 + lane < nf ? WS_FX + (r0 * 20 + 64 + lane) * FS : BIGOFF)
                          : (lane < (gc == 13 ? 20 : 24) ? WS_FC + (gc * 8 + lane - 16) * FS : BIGOFF); }
      pv[3] = lane < (sr == 21 ? 16 : 48) ? WS_FO + (48 * sr + lane) * FS : BIGOFF;
      pv[4] = lane < (sr == 21 ? 27 : 54) ? WS_FG + (((sr * 96) >> 6) * 27 + lane) * FS : BIGOFF;
#pragma unroll
      for (int j = 0; j < 6; j++) PIN(pv[j]);
    }
    const u64 E = loadE();
    const unsigned elo = (unsigned)E, ehi = (unsigned)(E >> 32);
    auto early_poll = [&](int j) { if (lw == 0) dma_sc1(ldsbase + L_POLL, pv[j], rWS, 0); };
    auto check = [&](int j) {
      const u32x2 f = *(volatile LAS u32x2*)(lds + L_POLL + lane * 16);
      return (bool)__all(pv[j] == BIGOFF || (f.x == elo && f.y == ehi));
    };
    auto spin = [&](int j) {
      for (unsigned it = 0; it < SPIN_CAP; it++) {
        u32x2 f;
        asm volatile("s_nop 4\n\tbuffer_load_dwordx2 %0, %1, %2, 0 offen sc1\n\ts_waitcnt vmcnt(0)" : "=&v"(f) : "v"(pv[j]), "s"(rWS) : "memory");
        if (__all(pv[j] == BIGOFF || (f.x == elo && f.y == ehi))) break;
        __builtin_amdgcn_s_sleep(PSLEEP);
      }
    };
    auto proA = [&](rsrc_t rA, int ncg, int tile0, int ntile, int k, int nk, int nq) {
      unsigned sl = slot;
#pragma unroll 1
      for (int q = 0; q < nq; q++) {
        unsigned so = (tile0 * ncg + 2 * k) * 1024, m = ldsbase + sl;
#pragma unroll 1
        for (int t = 0; t < ntile; t++) { dma2s(m, l16, l16, rA, so); m += 2048; so += ncg * 1024; }
        sl = nx(sl); k = (k + 1 == nk) ? 0 : k + 1;
      }
    };
    auto jobS = [&](auto NPT, auto EARLYT, bool prev, int j, int nextj, int nk, int k0, rsrc_t rA, int ncg, unsigned (&va)[3], auto issB) {
      constexpr int NP = decltype(NPT)::value; constexpr bool EARLY = decltype(EARLYT)::value;
      const int k1 = (k0 + 1 == nk) ? 0 : k0 + 1, k2 = (k1 + 1 == nk) ? 0 : k1 + 1;
      const unsigned s1 = nx(slot), s2 = nx(s1), ao = 6 * sr * ncg * 1024;
      auto iss = [&]() {
        dma3s(mAs + slot, va[0], va[1], va[2], rA, ao + kc * 2048);
        issB(slot, kc);
        kc = (kc + 1 == nk) ? 0 : kc + 1; slot = nx(slot);
      };
      if (prev) STEP(2 * NP);
      int c0 = 0;
      if (EARLY && check(j)) {
        kc = k0;
        iss(); STEP(NP + 5); iss(); STEP(10); iss();
        STAMP(); STAMP();
      } else {
        issB(slot, k0);
        if (prev) { STEP(NP + 2); early_poll(j); }
        issB(s1, k1);
        if (prev) STEP(2);
        issB(s2, k2);
        STAMP();
        const bool ok = prev && check(j);
        if (ok) {
          dma3s(mAs + slot, va[0], va[1], va[2], rA, ao + k0 * 2048);
          dma3s(mAs + s1, va[0], va[1], va[2], rA, ao + k1 * 2048);
          dma3s(mAs + s2, va[0], va[1], va[2], rA, ao + k2 * 2048);
        } else if (lw == 0) { spin(j); proA(rA, ncg, 6 * sr, 6, k0, nk, 3); }
        else waitv<0>();
        STAMP();
        slot = nx(s2); kc = (k2 + 1 == nk) ? 0 : k2 + 1;
        STEP2(ok, 6, 24); iss();
        STEP2(ok, 8, 17); iss();
        c0 = 2;
      }
#pragma unroll 1
      for (int c = c0; c < nk - 3; c++) { STEP(10); if (nextj >= 0 && c == nk - 5) early_poll(nextj); iss(); }
    };
    if (hasS) {
      STEP(5); iss1();
#pragma unroll 1
      for (int c = 1; c < 7; c++) { STEP(10); iss1(); }
      STEP(10); if (hasQ) issQ();
      STEP2(hasQ, 13, 5);
      STEP2(hasQ, 8, 0);
      if (hasQ) { issQ(); issQ(); } else { bar(); slot = 0; }
    }
    CODE_TOUCH(2);
    STAMP();
    if (hasQ) {
#pragma unroll 1
      for (int c = 0; c < 7; c++) { STEP(16); issQ(); }
      if (!hasS) {
        STEP(16); STEP(8); STEP(0); slot = 2 * SLOT;
        if (li >= 28 && (WARM & 2)) {
          touch(ldsbase + L_DUM, t50, mkr(wo, D_ * D_ * 2), pid * 50 * 128);
          for (int i = 0; i < 3; i++) touch(ldsbase + L_DUM, t45, mkr(w4, F_ * D_ * 2), (i * 128 + pid) * 45 * 128);
        }
      }
    }
    if (hasS) {
      const rsrc_t rW2 = mkr(w2, D_ * D_ * 2);
      jobS(IC<8>{}, IC<0>{}, hasQ, 0, -1, 10, sc, mkr(ws + WS_H, S_ * D_ * 2), 20, fa,
           [&](unsigned sl, int k) { dma2(mBs + sl, sp2[0], sp2[1], rW2, sBo + k * (64 * D_ * 2)); });
      STEP(10); STEP(5); early_poll(1); STEP(0);
    }
    CODE_TOUCH(4);
    STAMP();
    const rsrc_t rW3 = mkr(w3, D_ * F_ * 2);
    const unsigned gbo = gc * 256;
    const int kg0 = (gc + ROTG * (gr & 1)) % 10, kg1 = (kg0 + 1) % 10, kg2 = (kg0 + 2) % 10;
    {
      const unsigned s1 = nx(slot), s2 = nx(s1), s3 = nx(s2);
      if (hasG) dma4(mBb + s2, bg[0], bg[1], bg[2], bg[3], rW3, gbo + kg0 * (64 * F_ * 2));
      if (lw == 0) {
        if (!(hasS && check(1))) spin(1);
        const rsrc_t rQ = mkr(ws + WS_QKV, S_ * Q_ * 2);
        unsigned m = ldsbase + slot, so = (aT * 60 + 10 * ahh) * 1024;
#pragma unroll 1
        for (int t = 0; t < 5; t++) { dma2s(m, l16, l16, rQ, so); m += 2048; so += 2048; }
        so = (20 + 10 * ahh) * 1024 - 512;
#pragma unroll 1
        for (int t = 0; t < 10; t++) { dma2s(m, kv0, kv1, rQ, so); m += 2048; so += 1024; }
        m = ldsbase + s1; so = (40 + 10 * ahh) * 1024 - 512;
#pragma unroll 1
        for (int t = 0; t < 10; t++) { dma2s(m, kv0, kv1, rQ, so); m += 2048; so += 1024; }
      }
      STAMP();
      STEP(20);
      if (hasG) {
        if (lw == 1) { dma_sc1(ldsbase + L_POLL, pv[2], rWS, 0); dma_sc1(ldsbase + L_POLL + 1024, pv[5], rWS, 0); }
        dma4(mBb + s3, bg[0], bg[1], bg[2], bg[3], rW3, gbo + kg1 * (64 * F_ * 2));
      }
      STEP2(hasG, 4, 0);
    }
    STAMP();
    if (!hasG) bar();
    else {
      const rsrc_t rX1 = mkr(ws + WS_X1, S_ * D_ * 2);
      const unsigned gao = 8 * gr * 20 * 1024, sa0 = slot;
      slot = nx(nx(slot));
      bool ok;
      { const u32x2 f = *(volatile LAS u32x2*)(lds + L_POLL + lane * 16), g = *(volatile LAS u32x2*)(lds + L_POLL + 1024 + lane * 16);
        ok = __all((pv[2] == BIGOFF || (f.x == elo && f.y == ehi)) && (pv[5] == BIGOFF || (g.x == elo && g.y == ehi))); }
      if (ok) {
        dma4s(mAb + slot, fb[0], fb[1], fb[2], fb[3], rX1, gao + kg0 * 2048);
        dma4s(mAb + nx(slot), fb[0], fb[1], fb[2], fb[3], rX1, gao + kg1 * 2048);
      } else if (lw == 0) {
        for (unsigned it = 0; it < SPIN_CAP; it++) {
          u32x2 f, g;
          asm volatile("s_nop 4\n\tbuffer_load_dwordx2 %0, %2, %4, 0 offen sc1\n\tbuffer_load_dwordx2 %1, %3, %4, 0 offen sc1\n\ts_waitcnt vmcnt(0)"
                       : "=&v"(f), "=&v"(g) : "v"(pv[2]), "v"(pv[5]), "s"(rWS) : "memory");
          if (__all((pv[2] == BIGOFF || (f.x == elo && f.y == ehi)) && (pv[5] == BIGOFF || (g.x == elo && g.y == ehi)))) break;
          __builtin_amdgcn_s_sleep(PSLEEP);
        }
        proA(rX1, 20, 8 * gr, 8, kg0, 10, 2);
      } else waitv<0>();
      dma4(mBb + sa0, bg[0], bg[1], bg[2], bg[3], rW3, gbo + kg2 * (64 * F_ * 2));
      STAMP();
      int kg = (kg0 + 3) % 10;
      auto iss = [&]() {
        dma4s(mAb + slot, fb[0], fb[1], fb[2], fb[3], rX1, gao + kg * 2048);
        dma4(mBb + slot, bg[0], bg[1], bg[2], bg[3], rW3, gbo + kg * (64 * F_ * 2));
        kg = (kg + 1 == 10) ? 0 : kg + 1; slot = nx(slot);
      };
      STEP2(ok, 8, 20);
      dma4s(mAb + sa0, fb[0], fb[1], fb[2], fb[3], rX1, gao + kg2 * 2048);
      slot = nx(sa0);
      iss();
#pragma unroll
      for (int i = 0; i < 5; i++) { const int pi = lw + 4 * i; dma_sc1(ldsbase + L_ST + pi * 1024, l16, rWS, WS_ST + pi * ST_LD + gr * 1024); }
      dma_sc1(ldsbase + L_CS + lw * 1024, lane < 32 ? l16 : BIGOFF, rWS, WS_CS + lw * 8192 + gc * 512);
      STEP(22); iss();
      STEP(22); iss();
      STEP(16); iss();
      {
        const int row = lw * 32 + (lane & 31);
        float s = 0.f, q = 0.f;
#pragma unroll
        for (int pi = 0; pi < 10; pi++) { const f32x2 t = *(const f32x2*)(lds + L_ST + ((lane >> 5) * 10 + pi) * 1024 + row * 8); s += t.x; q += t.y; }
        const char* cp = lds + L_CS + (lane >> 5) * 2048 + row * 4;
        const float cs = sum32(*(const float*)cp + *(const float*)(cp + 1024));
        s = sum32(s); q = sum32(q);
        const float mean = s * (1.0f / D_), var = fmaxf(q * (1.0f / D_) - mean * mean, 0.f) + 1e-5f, rstd = __builtin_amdgcn_rsqf(var);
        if (lane < 32) *(f32x4*)(lds + L_RC + row * 16) = (f32x4){mean, GK1 * rstd, GK2 * rstd * rstd * rstd, var * rstd};
        else *(float*)(lds + L_CF + row * 4) = cs;
      }
#pragma unroll 1
      for (int c = 4; c < 7; c++) { STEP(16); iss(); }
      LG0;
      if (!hasS) { STEP(16); STEP(8); STEP(0); }
    }
    if (hasS) {
      const rsrc_t rWO = mkr(wo, D_ * D_ * 2), rW4 = mkr(w4, F_ * D_ * 2);
      jobS(IC<8>{}, IC<0>{}, hasG, 3, 4, 10, sc, mkr(ws + WS_O, S_ * D_ * 2), 20, fa,
           [&](unsigned sl, int k) {
             const int hf = k >= 5, c = k - 5 * hf; const bool tl = c == 4;
             dma2(mBs + sl, tl ? ob[0] : oa[0], tl ? ob[1] : oa[1], rWO, sBo + (hf * 320 + (tl ? 0 : (c & 1) * 160 + (c >> 1) * 8)) * (D_ * 2)); });
      jobS(IC<5>{}, IC<1>{}, true, 4, -1, 27, (sc * 5) % 27, mkr(ws + WS_H2, S_ * F_ * 2), 54, fh,
           [&](unsigned sl, int k) { dma2(mBs + sl, sp2[0], sp2[1], rW4, sBo + k * (64 * D_ * 2)); });
      STEP(10); STEP(5); STEP(0);
    }
    STAMP();
    asm volatile(".Lend_ld:");
  } else {
    const int wr = w >> 1, wc = w & 1;
    const int m16 = lane & 15, G = lane >> 4;
    __builtin_amdgcn_s_setprio(CPRIO);
    const u64 E = loadE();
    if (w == 3) {
      if (lane < 4) *(LAS unsigned*)(lds + L_ONE + lane * 4) = 0x3F803F80u;
      else if (lane < 8) *(LAS unsigned*)(lds + L_ONE + lane * 4) = 0u;
      asm volatile("s_waitcnt lgkmcnt(0)" ::: "memory");
    }
    unsigned pend = 0, idle = 1;
    auto putflag = [&](unsigned foff) { if (lane == 0) __hip_atomic_store((GAS u64*)(ws + foff), E, __ATOMIC_RELAXED, __HIP_MEMORY_SCOPE_AGENT); };
    auto flush = [&]() { if (!idle) { waitv<0>(); putflag(pend); idle = 1; } };
    auto tryflush = [&]() {
      unsigned st;
      asm volatile("s_getreg_b32 %0, hwreg(HW_REG_IB_STS)" : "=s"(st) :: "memory");
      if (__builtin_expect(((st & 0xC0000Fu) | idle) == 0, 0)) { waitv<0>(); putflag(pend); idle = 1; }
    };
#define PINV(v) asm volatile("" : "+v"(v))
#define LP(T, a) ((const LAS T*)(size_t)(a))
    const int q4 = (lane & 15) >> 2, p4 = lane & 3;
    const int bx = (((q4 >> 1) & 1) << 2) | ((G & 1) << 1);
    const unsigned sA0 = (G ^ ((m16 >> 1) & 7)) * 16, sA1 = ((4 + G) ^ ((m16 >> 1) & 7)) * 16;
    const unsigned brow = ldsbase + BOFF + (8 * G + q4) * 128 + 8 * (p4 & 1);
    const int cst = (G & 1) * 16 + (G >> 1) * 8;

    f32x4 acc[3][2];
    f32x4 aS[3], aQ[3], aC[2];
    u32x2 xt[3][2];
    float gate[3];
    auto small = [&](auto NCHT, auto STATT, auto S0T, int rot, auto hook) {
      constexpr int NCH = decltype(NCHT)::value, S0 = decltype(S0T)::value; constexpr bool STAT = decltype(STATT)::value;
      unsigned pa0[2], pa1[2], pb0[2], pb1[2];
      pa0[0] = ldsbase + (STAT ? (wr * 48 + m16) * 128 + sA0 : wr * 6144 + lane * 16);
      pa1[0] = ldsbase + (STAT ? (wr * 48 + m16) * 128 + sA1 : wr * 6144 + lane * 16 + 1024);
      pb0[0] = brow + (((wc * 4 + 0 + (p4 >> 1)) ^ bx) * 16); pb1[0] = brow + (((wc * 4 + 2 + (p4 >> 1)) ^ bx) * 16);
      pa0[1] = pa0[0] + 2 * SLOT; pa1[1] = pa1[0] + 2 * SLOT; pb0[1] = pb0[0] + 2 * SLOT; pb1[1] = pb1[0] + 2 * SLOT;
      PINV(pa0[1]); if (STAT) PINV(pa1[1]); else { pa1[0] = pa0[0] + 1024; pa1[1] = pa0[1] + 1024; }
      PINV(pb0[1]); PINV(pb1[1]);
#pragma unroll
      for (int i = 0; i < 3; i++)
#pragma unroll
        for (int j = 0; j < 2; j++) acc[i][j] = (f32x4){0.f, 0.f, 0.f, 0.f};
      const s16x8 ones = {0x3F80, 0x3F80, 0x3F80, 0x3F80, 0x3F80, 0x3F80, 0x3F80, 0x3F80};
      unsigned spbase = 0, spstep = 0; int kc = rot;
      if (STAT) {
#pragma unroll
        for (int i = 0; i < 3; i++) { aS[i] = (f32x4){0.f, 0.f, 0.f, 0.f}; aQ[i] = (f32x4){0.f, 0.f, 0.f, 0.f}; }
        aC[0] = aC[1] = (f32x4){0.f, 0.f, 0.f, 0.f};
        const int c3 = m16 & 3;
        spbase = ldsbase + (c3 == 1 ? L_GW + G * 16 : (c3 == 0 ? L_ONE : L_ONE + 16));
        spstep = c3 == 1 ? 64 : 0;
      }
      auto rd = [&](FragS& Fr, int c) {
        const int sl = (S0 + c) & 3, hi = sl >> 1; const unsigned so = (sl & 1) * SLOT;
#pragma unroll
        for (int ks = 0; ks < 2; ks++) {
          s16x4 l0 = __builtin_amdgcn_ds_read_tr16_b64_v4i16((LAS s16x4*)(size_t)(pb0[hi] + so + ks * 4096));
          s16x4 h0 = __builtin_amdgcn_ds_read_tr16_b64_v4i16((LAS s16x4*)(size_t)(pb0[hi] + so + ks * 4096 + 512));
          s16x4 l1 = __builtin_amdgcn_ds_read_tr16_b64_v4i16((LAS s16x4*)(size_t)(pb1[hi] + so + ks * 4096));
          s16x4 h1 = __builtin_amdgcn_ds_read_tr16_b64_v4i16((LAS s16x4*)(size_t)(pb1[hi] + so + ks * 4096 + 512));
          Fr.b[ks][0] = __builtin_shufflevector(l0, h0, 0, 1, 2, 3, 4, 5, 6, 7);
          Fr.b[ks][1] = __builtin_shufflevector(l1, h1, 0, 1, 2, 3, 4, 5, 6, 7);
#pragma unroll
          for (int i = 0; i < 3; i++) Fr.a[ks][i] = *LP(s16x8, (ks ? pa1[hi] : pa0[hi]) + so + i * 2048);
          if (STAT) Fr.sp[ks] = *LP(s16x8, spbase + spstep * (2 * kc + ks));
        }
        if (STAT) kc = (kc + 1 == NCH) ? 0 : kc + 1;
      };
      auto mm = [&](FragS& Fr) {
#pragma unroll
        for (int ks = 0; ks < 2; ks++) {
#pragma unroll
          for (int i = 0; i < 3; i++)
#pragma unroll
            for (int j = 0; j < 2; j++) acc[i][j] = __builtin_amdgcn_mfma_f32_16x16x32_bf16(Fr.b[ks][j], Fr.a[ks][i], acc[i][j], 0, 0, 0);
          if (STAT) {
#pragma unroll
            for (int i = 0; i < 3; i++) {
              aS[i] = __builtin_amdgcn_mfma_f32_16x16x32_bf16(Fr.sp[ks], Fr.a[ks][i], aS[i], 0, 0, 0);
              aQ[i] = __builtin_amdgcn_mfma_f32_16x16x32_bf16(Fr.a[ks][i], Fr.a[ks][i], aQ[i], 0, 0, 0);
            }
#pragma unroll
            for (int j = 0; j < 2; j++) aC[j] = __builtin_amdgcn_mfma_f32_16x16x32_bf16(Fr.b[ks][j], ones, aC[j], 0, 0, 0);
          }
        }
      };
      auto sched = [&]() {
#pragma unroll
        for (int i = 0; i < (STAT ? 8 : 7); i++) { SGB(0x8, 1, 0); SGB(0x100, 2, 0); }
        SGB(0x8, STAT ? 20 : 5, 0);
      };
      FragS F0, F1;
      bar(); SB0;
      if (STAT) {
#pragma unroll
        for (int i = 0; i < 3; i++)
#pragma unroll
          for (int j = 0; j < 2; j++)
            xt[i][j] = *LP(u32x2, ldsbase + S0 * SLOT + (wr * 48 + 16 * i + m16) * 128 + (((wc * 4 + 2 * j + (G >> 1)) ^ ((m16 >> 1) & 7)) * 16) + (G & 1) * 8);
      }
      rd(F0, 0); SB0;
      if (STAT) waitv<0>();
      constexpr int NQ = (NCH - 1) / 4;
      auto quad = [&](int c) {
        LG0; bar(); SB0; rd(F1, 1); mm(F0); sched(); SB0; hook(c);
        LG0; bar(); SB0; rd(F0, 2); mm(F1); sched(); SB0; hook(c + 1);
        LG0; bar(); SB0; rd(F1, 3); mm(F0); sched(); SB0; hook(c + 2);
        LG0; bar(); SB0; rd(F0, 0); mm(F1); sched(); SB0; hook(c + 3);
      };
      if (NCH == 10 && !STAT) { quad(1); quad(5); }
      else {
#pragma unroll 1
        for (int q = 0; q < NQ; q++) quad(0);
      }
      LG0; bar(); SB0; rd(F1, 1); mm(F0); sched(); SB0;
      if (NCH == 27) { LG0; bar(); SB0; rd(F0, 2); mm(F1); sched(); SB0; mm(F0); }
      else mm(F1);
      SB0;
    };
    f32x4 bacc[4][4];
    auto big = [&](auto FNT, auto S0T, auto late) {
      constexpr bool FN = decltype(FNT)::value; constexpr int S0 = decltype(S0T)::value;
#pragma unroll
      for (int i = 0; i < 4; i++)
#pragma unroll
        for (int j = 0; j < 4; j++) bacc[i][j] = (f32x4){0.f, 0.f, 0.f, 0.f};
      unsigned a0[2], a1[2], bo[2][4];
      a0[0] = ldsbase + (FN ? wr * 8192 + lane * 16 : (wr * 64 + m16) * 128 + sA0);
      a1[0] = ldsbase + (FN ? wr * 8192 + lane * 16 + 1024 : (wr * 64 + m16) * 128 + sA1);
      a0[1] = a0[0] + 2 * SLOT; a1[1] = a1[0] + 2 * SLOT;
      PINV(a0[1]); if (!FN) PINV(a1[1]); else { a1[0] = a0[0] + 1024; a1[1] = a0[1] + 1024; }
#pragma unroll
      for (int j = 0; j < 4; j++) { bo[0][j] = brow + wc * 8192 + (((2 * j + (p4 >> 1)) ^ bx) * 16); bo[1][j] = bo[0][j] + 2 * SLOT; PINV(bo[1][j]); }
      auto rd = [&](FragB& Fr, int c, int ks) {
        const int sl = (S0 + c) & 3, hi = sl >> 1; const unsigned so = (sl & 1) * SLOT;
#pragma unroll
        for (int j = 0; j < 4; j++) {
          s16x4 lo = __builtin_amdgcn_ds_read_tr16_b64_v4i16((LAS s16x4*)(size_t)(bo[hi][j] + so + ks * 4096));
          s16x4 hh = __builtin_amdgcn_ds_read_tr16_b64_v4i16((LAS s16x4*)(size_t)(bo[hi][j] + so + ks * 4096 + 512));
          Fr.b[j] = __builtin_shufflevector(lo, hh, 0, 1, 2, 3, 4, 5, 6, 7);
        }
#pragma unroll
        for (int i = 0; i < 4; i++) Fr.a[i] = *LP(s16x8, (ks ? a1[hi] : a0[hi]) + so + i * 2048);
      };
      auto mm = [&](FragB& Fr) {
#pragma unroll
        for (int i = 0; i < 4; i++)
#pragma unroll
          for (int j = 0; j < 4; j++) bacc[i][j] = __builtin_amdgcn_mfma_f32_16x16x32_bf16(Fr.b[j], Fr.a[i], bacc[i][j], 0, 0, 0);
      };
      auto sched = [&]() {
#pragma unroll
        for (int i = 0; i < 12; i++) { SGB(0x8, 1, 0); SGB(0x100, 1, 0); }
        SGB(0x8, 4, 0);
      };
      FragB F0, F1;
      auto one = [&](int c) {
        rd(F1, c, 1); mm(F0); sched(); SB0;
        LG0; bar(); SB0;
        rd(F0, c + 1, 0); mm(F1); sched(); SB0;
      };
      bar(); SB0;
      rd(F0, 0, 0); SB0;
#pragma unroll 1
      for (int q = 0; q < 2; q++) { one(0); one(1); tryflush(); one(2); one(3); tryflush(); }
      one(0);
      rd(F1, 1, 1); mm(F0); sched(); SB0;
      late(); SB0;
      mm(F1); SB0;
    };
    auto nohook = [&](int) {};
    const int scg = 2 * sc + wc;
    const int svt = 6 * sr + 3 * wr;

    auto colsum = [&]() {
      const int cu = 4 * xg + (li - 28);
      if (li >= 28 && cu < 27) {
        float cs[8];
#pragma unroll
        for (int e = 0; e < 8; e++) cs[e] = 0.f;
        const char* src = (const char*)w3 + (size_t)(w * 160 + l3) * (F_ * 2) + cu * 128 + (lane & 7) * 16;
        u32x4 v[20];
#pragma unroll
        for (int i = 0; i < 20; i++) v[i] = *(const u32x4*)(src + (size_t)i * 8 * (F_ * 2));
#pragma unroll
        for (int i = 0; i < 20; i++) {
#pragma unroll
          for (int e = 0; e < 4; e++) { cs[2 * e] += bflo(v[i][e]); cs[2 * e + 1] += bfhi(v[i][e]); }
        }
#pragma unroll
        for (int e = 0; e < 8; e++) { cs[e] += __shfl_xor(cs[e], 8); cs[e] = sum32(sum16(cs[e])); }
        if (lane < 16) {
          const bool hi = lane >= 8;
          const u32x4 sv = {__float_as_uint(hi ? cs[4] : cs[0]), __float_as_uint(hi ? cs[5] : cs[1]), __float_as_uint(hi ? cs[6] : cs[2]), __float_as_uint(hi ? cs[7] : cs[3])};
          __builtin_amdgcn_raw_buffer_store_b128(sv, rWS, WS_CS + w * 8192 + cu * 256 + (lane & 7) * 32 + (lane >> 3) * 16, 0, 16);
        }
        pend = WS_FC + (cu * 4 + w) * FS; idle = 0;
      }
    };
    if (hasS) {
      small(IC<10>{}, IC<1>{}, IC<0>{}, sc, nohook);
      STAMP();
      const rsrc_t rH = mkr(ws + WS_H, S_ * D_ * 2);
      const int sh = scg < 5 ? 1 : (scg < 10 ? -1 : (scg < 15 ? 2 : -2));
#pragma unroll
      for (int i = 0; i < 3; i++) {
        const float mean = aS[i][0] * (1.0f / D_);
        const int r = m16 & 3;
        float q = r == 0 ? aQ[i][0] : (r == 1 ? aQ[i][1] : (r == 2 ? aQ[i][2] : aQ[i][3]));
        q = sum32(sum16(G == (m16 >> 2) ? q : 0.f));
        const float var = fmaxf(q * (1.0f / D_) - mean * mean, 0.f) + 1e-5f;
        const float rstd = __builtin_amdgcn_rsqf(var), gs = var * rstd, ga = GK1 * rstd, gb = GK2 * rstd * rstd * rstd;
        gate[i] = __builtin_amdgcn_rcpf(1.0f + __builtin_amdgcn_exp2f(-1.4426950408889634f * aS[i][1]));
        const f32x4 z0 = acc[i][0] - aC[0] * mean, z1 = acc[i][1] - aC[1] * mean;
        const u32x4 pk = {gelu_pk(z0.xy, ga, gb, gs), gelu_pk(z0.zw, ga, gb, gs), gelu_pk(z1.xy, ga, gb, gs), gelu_pk(z1.zw, ga, gb, gs)};
        const int r0 = vrow((svt + i) * 16 + m16), r1 = r0 + sh, v1 = rowv(r1);
        const unsigned off = (r0 < S_ && r1 >= 0) ? (unsigned)(((v1 >> 4) * 20 + scg) * 1024 + ((v1 & 15) + 16 * G) * 16) : BIGOFF;
        __builtin_amdgcn_raw_buffer_store_b128(pk, rH, off, 0, 16);
      }
      if (wr == 0 && sr == (sh > 0 ? 0 : 21)) {
        const int v1 = rowv(sh > 0 ? m16 : S_ - 1 - m16);
        const unsigned off = m16 < abs(sh) ? (unsigned)(((v1 >> 4) * 20 + scg) * 1024 + ((v1 & 15) + 16 * G) * 16) : BIGOFF;
        __builtin_amdgcn_raw_buffer_store_b128((u32x4){0u, 0u, 0u, 0u}, rH, off, 0, 16);
      }
      pend = WS_FH + ((2 * sr + wr) * 20 + scg) * FS; idle = 0;
      if (!hasQ) { flush(); bar(); }
    } else if (!CSLATE) colsum();
    STAMP();
    u32x4 qpk[8];
    const rsrc_t rQ = mkr(ws + WS_QKV, S_ * Q_ * 2);
    const unsigned qoff = ((8 * qr + 4 * wr) * 60 + 4 * qc + 2 * wc) * 1024 + (cst * 2 + m16) * 16;
    const unsigned qflag = WS_FQ + ((2 * qr + wr) * 30 + 2 * qc + wc) * FS;
    auto qstore = [&](int k) { __builtin_amdgcn_raw_buffer_store_b128(qpk[k], rQ, qoff + (k >> 1) * (60 * 1024) + (k & 1) * 1024, 0, 16); };
    if (hasQ) {
      big(IC<0>{}, IC<2>{}, [&]() {});
      STAMP();
      flush();
#pragma unroll
      for (int i = 0; i < 4; i++)
#pragma unroll
        for (int jp = 0; jp < 2; jp++) {
          const f32x4 u = bacc[i][2 * jp], v = bacc[i][2 * jp + 1];
          qpk[2 * i + jp] = pack2(cvt_pk(u[0], u[1]), cvt_pk(u[2], u[3]), cvt_pk(v[0], v[1]), cvt_pk(v[2], v[3]));
        }
#pragma unroll
      for (int k = 0; k < 8; k++) qstore(k);
      pend = qflag; idle = 0;
      if (!hasS) { flush(); if (CSLATE) { colsum(); flush(); } }
    }
    STAMP();
    if (hasS) {
      small(IC<10>{}, IC<0>{}, IC<0>{}, 0, [&](int c) { if (c >= 2) tryflush(); });
      STAMP();
      const rsrc_t rX1 = mkr(ws + WS_X1, S_ * D_ * 2);
      float ssum = 0.f, ssq = 0.f;
#pragma unroll
      for (int i = 0; i < 3; i++) {
        float s = 0.f, q = 0.f;
#pragma unroll
        for (int j = 0; j < 2; j++) {
          const unsigned p0 = cvt_pk(bflo(xt[i][j].x) + acc[i][j][0], bfhi(xt[i][j].x) + acc[i][j][1]);
          const unsigned p1 = cvt_pk(bflo(xt[i][j].y) + acc[i][j][2], bfhi(xt[i][j].y) + acc[i][j][3]);
          xt[i][j] = (u32x2){p0, p1};
          s = dot2(p0, 0x3F803F80u, s); s = dot2(p1, 0x3F803F80u, s);
          q = dot2(p0, p0, q); q = dot2(p1, p1, q);
        }
        __builtin_amdgcn_raw_buffer_store_b128((u32x4){xt[i][0].x, xt[i][0].y, xt[i][1].x, xt[i][1].y}, rX1, ((svt + i) * 20 + scg) * 1024 + lane * 16, 0, 16);
        s = sum32(sum16(s)); q = sum32(sum16(q));
        if (G == i) { ssum = s; ssq = q; }
      }
      if (G < 3) {
        u32x2 sv = {__float_as_uint(ssum), __float_as_uint(ssq)};
        __builtin_amdgcn_raw_buffer_store_b64(sv, rWS, WS_ST + scg * ST_LD + (svt * 16 + lane) * 8, 0, 16);
      }
      waitv<0>();
      putflag(WS_FX + ((2 * sr + wr) * 20 + scg) * FS);
      flush();
    }
    STAMP();
    {
      const int hl = 4 * w + G, pf = (5 * hl + 1) >> 1;
      const int tl = (hl & 1) ? -1 : 2, th = (hl & 1) * 8;
      const char* s0p = lds + 2 * SLOT;
      const char* s1p = lds + 3 * SLOT;
      bar(); SB0;
      STAMP();
      const char* qp = s0p + pf * 256 + m16 * 16;
      const u32x4 q0 = *(const u32x4*)qp, q1 = *(const u32x4*)(qp + 256);
      const u32x2 q2 = *(const u32x2*)(qp + tl * 256 + th);
      float lg[8];
      const int srow_g = vrow(aT * 16 + m16);
      const char* kp = s0p + 10240 + pf * 512 + (9 + m16) * 16;
      const char* kt = kp + tl * 512 + th;
#pragma unroll
      for (int ww = 0; ww < 8; ww++) {
        const u32x4 k0 = *(const u32x4*)(kp + (7 - ww) * 16), k1 = *(const u32x4*)(kp + (7 - ww) * 16 + 512);
        const u32x2 k2 = *(const u32x2*)(kt + (7 - ww) * 16);
        float a0 = 0.f, a1 = 0.f;
#pragma unroll
        for (int d = 0; d < 4; d++) { a0 = dot2(q0[d], k0[d], a0); a1 = dot2(q1[d], k1[d], a1); }
        a0 = dot2(q2.x, k2.x, a0); a1 = dot2(q2.y, k2.y, a1);
        lg[ww] = (srow_g - 2 * ww >= 0) ? (a0 + a1) * (0.22360679774997896f * 1.4426950408889634f) : -1e30f;
      }
      float mx = lg[0];
#pragma unroll
      for (int ww = 1; ww < 8; ww++) mx = fmaxf(mx, lg[ww]);
      float sum = 0.f;
#pragma unroll
      for (int ww = 0; ww < 8; ww++) { lg[ww] = __builtin_amdgcn_exp2f(lg[ww] - mx); sum += lg[ww]; }
      const float inv = __builtin_amdgcn_rcpf(sum);
      unsigned pp[4];
#pragma unroll
      for (int k = 0; k < 4; k++) pp[k] = cvt_pk(lg[2 * k] * inv, lg[2 * k + 1] * inv);
      SB0; STAMP(); bar(); SB0;
      float o[20];
#pragma unroll
      for (int d = 0; d < 20; d++) o[d] = 0.f;
      const char* vp = s1p + pf * 512 + (9 + m16) * 16;
      const char* vt = vp + tl * 512 + th;
#pragma unroll
      for (int k = 0; k < 4; k++) {
        unsigned ta[10], tb[10];
        { const u32x4 t0 = *(const u32x4*)(vp + (7 - 2 * k) * 16), t1 = *(const u32x4*)(vp + (7 - 2 * k) * 16 + 512); const u32x2 t2 = *(const u32x2*)(vt + (7 - 2 * k) * 16);
          ta[0] = t0.x; ta[1] = t0.y; ta[2] = t0.z; ta[3] = t0.w; ta[4] = t1.x; ta[5] = t1.y; ta[6] = t1.z; ta[7] = t1.w; ta[8] = t2.x; ta[9] = t2.y; }
        { const u32x4 t0 = *(const u32x4*)(vp + (6 - 2 * k) * 16), t1 = *(const u32x4*)(vp + (6 - 2 * k) * 16 + 512); const u32x2 t2 = *(const u32x2*)(vt + (6 - 2 * k) * 16);
          tb[0] = t0.x; tb[1] = t0.y; tb[2] = t0.z; tb[3] = t0.w; tb[4] = t1.x; tb[5] = t1.y; tb[6] = t1.z; tb[7] = t1.w; tb[8] = t2.x; tb[9] = t2.y; }
#pragma unroll
        for (int d = 0; d < 10; d++) {
          o[2 * d + 0] = dot2(__builtin_amdgcn_perm(tb[d], ta[d], 0x05040100u), pp[k], o[2 * d + 0]);
          o[2 * d + 1] = dot2(__builtin_amdgcn_perm(tb[d], ta[d], 0x07060302u), pp[k], o[2 * d + 1]);
        }
      }
      STAMP();
      const rsrc_t rO = mkr(ws + WS_O, S_ * D_ * 2);
      const unsigned ooff = (aT * 20 + 10 * ahh) * 1024;
      __builtin_amdgcn_raw_buffer_store_b128((u32x4){cvt_pk(o[0], o[1]), cvt_pk(o[2], o[3]), cvt_pk(o[4], o[5]), cvt_pk(o[6], o[7])}, rO, ooff + w * 1024 + lane * 16, 0, 16);
      __builtin_amdgcn_raw_buffer_store_b128((u32x4){cvt_pk(o[8], o[9]), cvt_pk(o[10], o[11]), cvt_pk(o[12], o[13]), cvt_pk(o[14], o[15])}, rO, ooff + (4 + w) * 1024 + lane * 16, 0, 16);
      {
        const unsigned t0 = cvt_pk(o[16], o[17]), t1 = cvt_pk(o[18], o[19]);
        auto r0 = __builtin_amdgcn_permlane16_swap(t0, t0, false, false);
        auto r1 = __builtin_amdgcn_permlane16_swap(t1, t1, false, false);
        __builtin_amdgcn_raw_buffer_store_b128((u32x4){r0[0], r1[0], r0[1], r1[1]}, rO,
                                               (G & 1) ? BIGOFF : ooff + 8 * 1024 + w * 512 + (G >> 1) * 256 + m16 * 16, 0, 16);
      }
      STAMP();
      pend = WS_FO + ((aT * 2 + ahh) * 4 + w) * FS; idle = 0;
      if (!hasG) { flush(); bar(); }
    }
    STAMP();
    if (hasG) {
      f32x4 rc[4], cs[4];
      big(IC<1>{}, IC<0>{}, [&]() {
#pragma unroll
        for (int i = 0; i < 4; i++) {
          rc[i] = *(const f32x4*)(lds + L_RC + (wr * 64 + 16 * i + m16) * 16);
          cs[i] = *(const f32x4*)(lds + L_CF + (wc * 64 + 16 * i + 4 * G) * 4);
        }
      });
      STAMP();
      flush();
      if (gc * 128 + wc * 64 < F_) {
        const rsrc_t rG = mkr(ws + WS_H2, S_ * F_ * 2);
        const unsigned goff = ((8 * gr + 4 * wr) * 54 + 4 * gc + 2 * wc) * 1024 + lane * 16;
#pragma unroll
        for (int i = 0; i < 4; i++)
#pragma unroll
          for (int jp = 0; jp < 2; jp++) {
            const f32x4 z0 = bacc[i][2 * jp] - cs[2 * jp] * rc[i].x, z1 = bacc[i][2 * jp + 1] - cs[2 * jp + 1] * rc[i].x;
            const u32x4 pk = {gelu_pk(z0.xy, rc[i].y, rc[i].z, rc[i].w), gelu_pk(z0.zw, rc[i].y, rc[i].z, rc[i].w),
                              gelu_pk(z1.xy, rc[i].y, rc[i].z, rc[i].w), gelu_pk(z1.zw, rc[i].y, rc[i].z, rc[i].w)};
            __builtin_amdgcn_raw_buffer_store_b128(pk, rG, goff + i * (54 * 1024) + jp * 1024, 0, 16);
          }
        pend = WS_FG + ((2 * gr + wr) * 27 + 2 * gc + wc) * FS; idle = 0;
      }
    }
    STAMP();
    if (hasS) {
      small(IC<10>{}, IC<0>{}, IC<2>{}, 0, [&](int c) { if (c <= 6) tryflush(); });
      STAMP();
      flush();
      f32x4 tacc[3][2];
#pragma unroll
      for (int i = 0; i < 3; i++)
#pragma unroll
        for (int j = 0; j < 2; j++) {
          const float g = gate[i], h = 1.0f - g;
          tacc[i][j] = (f32x4){g * acc[i][j][0] + h * bflo(xt[i][j].x), g * acc[i][j][1] + h * bfhi(xt[i][j].x),
                               g * acc[i][j][2] + h * bflo(xt[i][j].y), g * acc[i][j][3] + h * bfhi(xt[i][j].y)};
        }
      small(IC<27>{}, IC<0>{}, IC<0>{}, 0, nohook);
      STAMP();
      const rsrc_t rOut = mkr(out, S_ * D_ * 2);
#pragma unroll
      for (int i = 0; i < 3; i++) {
        const float h = 1.0f - gate[i];
        unsigned pk[2][2];
#pragma unroll
        for (int j = 0; j < 2; j++) {
          pk[j][0] = cvt_pk(tacc[i][j][0] + h * acc[i][j][0], tacc[i][j][1] + h * acc[i][j][1]);
          pk[j][1] = cvt_pk(tacc[i][j][2] + h * acc[i][j][2], tacc[i][j][3] + h * acc[i][j][3]);
        }
        __builtin_amdgcn_raw_buffer_store_b128(pack2(pk[0][0], pk[0][1], pk[1][0], pk[1][1]), rOut,
                                               vrow((svt + i) * 16 + m16) * (D_ * 2) + (scg * 32 + cst) * 2, 0, 0);
      }
    }
    flush();
    if (b == 255 && w == 0) {
      const unsigned elo = (unsigned)E, ehi = (unsigned)(E >> 32);
      for (unsigned spin = 0; spin < SPIN_CAP; spin++) {
        bool ok = true;
#pragma unroll
        for (int t = 0; t < 16; t++) {
          const u32x2 f = __builtin_amdgcn_raw_buffer_load_b64(rWS, WS_FO + (t * 64 + lane) * FS, 0, 16);
          ok = ok && f.x == elo && f.y == ehi;
        }
        if (__all(ok)) break;
        __builtin_amdgcn_s_sleep(4);
      }
      if (lane == 0) __hip_atomic_store((GAS u64*)(ws + WS_EPOCH), E, __ATOMIC_RELAXED, __HIP_MEMORY_SCOPE_AGENT);
    }
    STAMP();
    asm volatile(".Lend_cp:");
  }
}

extern "C" void kernel_launch(void* const* d_in, const int* in_sizes, int n_in, void* d_out, int out_size, void* d_ws, size_t ws_size,
                              hipStream_t stream) {
  (void)in_sizes; (void)n_in; (void)out_size; (void)ws_size;
  (void)hipFuncSetAttribute((const void*)fused_kernel, hipFuncAttributeMaxDynamicSharedMemorySize, LDS_BYTES);
  fused_kernel<<<256, 512, LDS_BYTES, stream>>>((const u16*)d_in[0], (const u16*)d_in[1], (const u16*)d_in[2], (const u16*)d_in[4],
                                                (const u16*)d_in[5], (const u16*)d_in[6], (const u16*)d_in[7], (const u16*)d_in[8],
                                                (u16*)d_out, (char*)d_ws);
}
```

```cpp
#include <hip/hip_runtime.h>
#include <hip/hip_bf16.h>

typedef unsigned short u16;
typedef unsigned long long u64;
typedef unsigned u32x4 __attribute__((ext_vector_type(4)));
typedef unsigned u32x2 __attribute__((ext_vector_type(2)));
typedef short s16x4 __attribute__((ext_vector_type(4)));
typedef short s16x8 __attribute__((ext_vector_type(8)));
typedef float f32x4 __attribute__((ext_vector_type(4)));
typedef float f32x2 __attribute__((ext_vector_type(2)));
typedef __bf16 bf16x2_t __attribute__((ext_vector_type(2)));
#define LAS __attribute__((address_space(3)))
#define GAS __attribute__((address_space(1)))
typedef __amdgpu_buffer_rsrc_t rsrc_t;

#define FS 256

constexpr int S_ = 2048, D_ = 640, F_ = 1728, Q_ = 1920;
constexpr unsigned WS_EPOCH = 0;
constexpr unsigned FSZ = 1024 * FS;
constexpr unsigned WS_FH   = 4096;
constexpr unsigned WS_FQ   = WS_FH + FSZ;
constexpr unsigned WS_FX   = WS_FQ + FSZ;
constexpr unsigned WS_FO   = WS_FX + FSZ;
constexpr unsigned WS_FG   = WS_FO + FSZ;
constexpr unsigned WS_FC   = WS_FG + FSZ;
constexpr unsigned WS_H    = WS_FC + FSZ;
constexpr unsigned WS_QKV  = WS_H + S_ * D_ * 2;
constexpr unsigned WS_X1   = WS_QKV + S_ * Q_ * 2;
constexpr unsigned WS_O    = WS_X1 + S_ * D_ * 2;
constexpr unsigned WS_H2   = WS_O + S_ * D_ * 2;
constexpr unsigned WS_ST   = WS_H2 + S_ * F_ * 2;
constexpr unsigned ST_LD = 2112 * 8;
constexpr unsigned WS_CS   = WS_ST + 20 * ST_LD;
constexpr unsigned WS_END  = WS_CS + 32768;
constexpr unsigned SLOT = 32768, BOFF = 16384;
constexpr unsigned L_ST = 131072, L_CS = L_ST + 20480, L_RC = L_CS + 4096, L_CF = L_RC + 2048, L_ONE = L_CF + 512;
constexpr unsigned L_GW = L_ST, L_1S = L_ST + 3072 + 64;
constexpr unsigned L_SP = L_ST;
constexpr unsigned L_POLL = L_ONE + 64, L_DUM = L_POLL + 2048;
constexpr unsigned LDS_BYTES = L_DUM + 256;
constexpr u64 EPOCH_STEP = 0x9E3779B97F4A7C15ull;
constexpr unsigned BIGOFF = 0x7fffff00u;
constexpr unsigned SPIN_CAP = 1u << 17;

template <int N> __device__ __forceinline__ void waitv() { asm volatile("s_waitcnt vmcnt(%0)" ::"i"(N) : "memory"); }
__device__ __forceinline__ void bar() { asm volatile("" ::: "memory"); __builtin_amdgcn_s_barrier(); asm volatile("" ::: "memory"); }
__device__ __forceinline__ unsigned cvt_pk(float lo, float hi) { f32x2 f = {lo, hi}; bf16x2_t b = __builtin_convertvector(f, bf16x2_t); return __builtin_bit_cast(unsigned, b); }
__device__ __forceinline__ float bflo(unsigned v) { return __uint_as_float(v << 16); }
__device__ __forceinline__ float bfhi(unsigned v) { return __uint_as_float(v & 0xffff0000u); }
__device__ __forceinline__ float dot2(unsigned a, unsigned b, float c) { return __builtin_amdgcn_fdot2_f32_bf16(__builtin_bit_cast(bf16x2_t, a), __builtin_bit_cast(bf16x2_t, b), c, false); }
__device__ __forceinline__ rsrc_t mkr(const void* p, unsigned bytes) { return __builtin_amdgcn_make_buffer_rsrc((void*)p, (short)0, (int)bytes, 0x00020000); }
__device__ __forceinline__ int vrow(int v) { return (v & ~31) + 2 * (v & 15) + ((v >> 4) & 1); }
__device__ __forceinline__ int rowv(int r) { return (r & ~31) + ((r & 1) << 4) + ((r & 31) >> 1); }
__device__ __forceinline__ float sum16(float a) { auto r = __builtin_amdgcn_permlane16_swap(__float_as_uint(a), __float_as_uint(a), false, false); return __uint_as_float(r[0]) + __uint_as_float(r[1]); }
__device__ __forceinline__ float sum32(float a) { auto r = __builtin_amdgcn_permlane32_swap(__float_as_uint(a), __float_as_uint(a), false, false); return __uint_as_float(r[0]) + __uint_as_float(r[1]); }
__device__ __forceinline__ void dma(unsigned m0v, unsigned vo, rsrc_t r, unsigned so) {
  asm volatile("s_mov_b32 m0, %0\n\ts_nop 3\n\tbuffer_load_dwordx4 %1, %2, %3 offen lds" ::"s"(m0v), "v"(vo), "s"(r), "s"(so) : "memory");
}
__device__ __forceinline__ void dma_sc1(unsigned m0v, unsigned vo, rsrc_t r, unsigned so) {
  asm volatile("s_mov_b32 m0, %0\n\ts_nop 3\n\tbuffer_load_dwordx4 %1, %2, %3 offen sc1 lds" ::"s"(m0v), "v"(vo), "s"(r), "s"(so) : "memory");
}
constexpr float GK1 = -2.0f * 0.7978845608028654f * 1.4426950408889634f, GK2 = GK1 * 0.044715f;
__device__ __forceinline__ u32x4 gelu8(f32x4 z0, f32x4 z1, float ga, float gb, float gs) {
  const f32x4 t0 = z0 * (z0 * z0 * gb + ga), t1 = z1 * (z1 * z1 * gb + ga);
  f32x4 e0, e1;
#pragma unroll
  for (int e = 0; e < 4; e++) { e0[e] = __builtin_amdgcn_exp2f(t0[e]); e1[e] = __builtin_amdgcn_exp2f(t1[e]); }
  e0 = e0 * gs + gs; e1 = e1 * gs + gs;
#pragma unroll
  for (int e = 0; e < 4; e++) { e0[e] = __builtin_amdgcn_rcpf(e0[e]); e1[e] = __builtin_amdgcn_rcpf(e1[e]); }
  e0 = z0 * e0; e1 = z1 * e1;
  return (u32x4){cvt_pk(e0[0], e0[1]), cvt_pk(e0[2], e0[3]), cvt_pk(e1[0], e1[1]), cvt_pk(e1[2], e1[3])};
}
__device__ __forceinline__ void dma2(unsigned m0v, unsigned v0, unsigned v1, rsrc_t r, unsigned so) {
  asm volatile("s_mov_b32 m0, %0\n\ts_nop 3\n\tbuffer_load_dwordx4 %1, %3, %4 offen lds\n\tbuffer_load_dwordx4 %2, %3, %4 offen offset:1024 lds"
               ::"s"(m0v), "v"(v0), "v"(v1), "s"(r), "s"(so) : "memory");
}
__device__ __forceinline__ void dma2s(unsigned m0v, unsigned v0, unsigned v1, rsrc_t r, unsigned so) {
  asm volatile("s_mov_b32 m0, %0\n\ts_nop 3\n\tbuffer_load_dwordx4 %1, %3, %4 offen sc1 lds\n\tbuffer_load_dwordx4 %2, %3, %4 offen offset:1024 sc1 lds"
               ::"s"(m0v), "v"(v0), "v"(v1), "s"(r), "s"(so) : "memory");
}
__device__ __forceinline__ void dma4(unsigned m0v, unsigned v0, unsigned v1, unsigned v2, unsigned v3, rsrc_t r, unsigned so) {
  asm volatile("s_mov_b32 m0, %0\n\ts_nop 3\n\tbuffer_load_dwordx4 %1, %5, %6 offen lds\n\tbuffer_load_dwordx4 %2, %5, %6 offen offset:1024 lds\n\t"
               "buffer_load_dwordx4 %3, %5, %6 offen offset:2048 lds\n\tbuffer_load_dwordx4 %4, %5, %6 offen offset:3072 lds"
               ::"s"(m0v), "v"(v0), "v"(v1), "v"(v2), "v"(v3), "s"(r), "s"(so) : "memory");
}
__device__ __forceinline__ void dma3s(unsigned m0v, unsigned v0, unsigned v1, unsigned v2, rsrc_t r, unsigned so) {
  asm volatile("s_mov_b32 m0, %0\n\ts_nop 3\n\tbuffer_load_dwordx4 %1, %4, %5 offen sc1 lds\n\tbuffer_load_dwordx4 %2, %4, %5 offen offset:1024 sc1 lds\n\t"
               "buffer_load_dwordx4 %3, %4, %5 offen offset:2048 sc1 lds"
               ::"s"(m0v), "v"(v0), "v"(v1), "v"(v2), "s"(r), "s"(so) : "memory");
}
__device__ __forceinline__ void dma4s(unsigned m0v, unsigned v0, unsigned v1, unsigned v2, unsigned v3, rsrc_t r, unsigned so) {
  asm volatile("s_mov_b32 m0, %0\n\ts_nop 3\n\tbuffer_load_dwordx4 %1, %5, %6 offen sc1 lds\n\tbuffer_load_dwordx4 %2, %5, %6 offen offset:1024 sc1 lds\n\t"
               "buffer_load_dwordx4 %3, %5, %6 offen offset:2048 sc1 lds\n\tbuffer_load_dwordx4 %4, %5, %6 offen offset:3072 sc1 lds"
               ::"s"(m0v), "v"(v0), "v"(v1), "v"(v2), "v"(v3), "s"(r), "s"(so) : "memory");
}
__device__ __forceinline__ void touch(unsigned m0v, unsigned vo, rsrc_t r, unsigned so) {
  asm volatile("s_mov_b32 m0, %0\n\ts_nop 3\n\tbuffer_load_dword %1, %2, %3 offen lds" ::"s"(m0v), "v"(vo), "s"(r), "s"(so) : "memory");
}
__device__ __forceinline__ u32x4 pack2(unsigned a0, unsigned a1, unsigned b0, unsigned b1) {
  auto r0 = __builtin_amdgcn_permlane16_swap(a0, b0, false, false);
  auto r1 = __builtin_amdgcn_permlane16_swap(a1, b1, false, false);
  return (u32x4){r0[0], r1[0], r0[1], r1[1]};
}

struct FragS { s16x8 a[2][3]; s16x8 b[2][2]; s16x8 sp[2]; };
struct FragB { s16x8 a[4]; s16x8 b[4]; };
#define SGB __builtin_amdgcn_sched_group_barrier
#define SB0 __builtin_amdgcn_sched_barrier(0)
#define LG0 __builtin_amdgcn_s_waitcnt(0xC07F)
#define KNAME "_Z12fused_kernelPKtS0_S0_S0_S0_S0_S0_S0_PtPc"
#define PGAP 3
#define ADELAY 4
#define PRESPIN 5
#define W4S 10
#define W4E 27
#define W3N 3
template <int N> using IC = std::integral_constant<int, N>;

typedef unsigned u32x16 __attribute__((ext_vector_type(16)));
__global__ __launch_bounds__(512, 2) void fused_kernel(const u16* __restrict__ x_, const u16* __restrict__ wqkv_, const u16* __restrict__ wo_,
                                                       const u16* __restrict__ w1_, const u16* __restrict__ w2_, const u16* __restrict__ w3_,
                                                       const u16* __restrict__ w4_, const u16* __restrict__ gw_, u16* __restrict__ out_, char* __restrict__ ws_) {
  extern __shared__ __attribute__((aligned(16))) char lds[];
  u32x16 ka; u32x4 kb; u64 pc_; unsigned cl1_, cl2_, pco_;
  asm volatile("s_load_dwordx16 %0, %6, 0x0\n\ts_load_dwordx4 %1, %6, 0x40\n\t"
               "s_getpc_b64 %2\n.Lpc_here:\n\ts_mov_b32 %3, .Lend_ld-" KNAME "\n\ts_mov_b32 %4, .Lend_cp-" KNAME "\n\ts_mov_b32 %5, .Lpc_here-" KNAME
               : "=&s"(ka), "=&s"(kb), "=&s"(pc_), "=&s"(cl1_), "=&s"(cl2_), "=&s"(pco_) : "s"(__builtin_amdgcn_kernarg_segment_ptr()) : "memory");
  SB0;
  const int tid = threadIdx.x, lane = tid & 63, w = __builtin_amdgcn_readfirstlane(tid >> 6);
  const int b = blockIdx.x, xg = b & 7, li = b >> 3;
  const unsigned ldsbase = (unsigned)(size_t)(LAS char*)lds;
  const rsrc_t rC = mkr((const char*)(pc_ - pco_), max(cl1_, cl2_));
  touch(ldsbase + L_DUM, lane < 2 ? (unsigned)((li * 8 + w) * 2 + lane) * 128u : BIGOFF, rC, 0);
  SB0;
  const int sstart = (55 * xg + 1) >> 1, scnt = ((55 * (xg + 1) + 1) >> 1) - sstart;
  const bool hasS = li < scnt;
  const int sj = sstart + li, sr = sj / 10, sc = sj - sr * 10;
  const int lw = w & 3, l3 = lane >> 3;
  auto voffX = [&](int p, int i) { return vrow(8 * p + l3) * (D_ * 2) + (((lane & 7) ^ (((p & 1) << 2) + (lane >> 4))) * 16) + (4 - i) * 1024; };
  auto bswz = [&](int pp, int i) { return (pp >> 3) * 128 + (((lane & 7) ^ ((((l3 >> 1) & 1) << 2) | ((pp & 1) << 1))) * 16) - i * 1024; };
  auto voffB = [&](int pp, int i, unsigned ld) { return (8 * (pp & 7) + l3) * ld + bswz(pp, i); };
  unsigned sa[3], sb2[2];
#pragma unroll
  for (int i = 0; i < 3; i++) sa[i] = voffX(3 * lw + i, i);
#pragma unroll
  for (int i = 0; i < 2; i++) sb2[i] = voffB(2 * lw + i, i, D_ * 2);
  const unsigned mAs = ldsbase + 3 * lw * 1024, mBs = ldsbase + BOFF + 2 * lw * 1024;
  asm volatile("s_waitcnt lgkmcnt(0)" : "+s"(ka), "+s"(kb) :: "memory");
#define KARG(v, i) ((char*)(((u64)v[2 * (i) + 1] << 32) | v[2 * (i)]))
  const char *x = KARG(ka, 0), *wqkv = KARG(ka, 1), *wo = KARG(ka, 2), *w1 = KARG(ka, 3), *w2 = KARG(ka, 4), *w3 = KARG(ka, 5), *w4 = KARG(ka, 6), *gw = KARG(ka, 7);
  char *out = KARG(kb, 0), *ws = KARG(kb, 1);
  const rsrc_t rX = mkr((const char*)x - 4096, S_ * D_ * 2 + 4096), rW1 = mkr(w1, D_ * D_ * 2);
  const unsigned sAo = sr * 96 * (D_ * 2), sBo = sc * 128;
  if (hasS) {
    const int k0 = w < 4 ? (sc == 9 ? 0 : sc + 1) : sc; const unsigned s0 = w < 4 ? SLOT : 0;
    dma3s(mAs + s0, sa[0], sa[1], sa[2], rX, sAo + k0 * 128);
    dma2(mBs + s0, sb2[0], sb2[1], rW1, sBo + k0 * (64 * D_ * 2));
  }
  auto loadE = [&]() {
    u64 e;
    asm volatile("s_load_dwordx2 %0, %1, 0x0\n\ts_waitcnt lgkmcnt(0)" : "=s"(e) : "s"(ws) : "memory");
    return e + EPOCH_STEP;
  };
  const bool hasQ = li >= 2;  const int qi = li - 2, qr = 2 * xg + qi / 15, qc = qi % 15;
  const bool hasG = li >= 4;  const int gi = li - 4, gr = 2 * xg + gi / 14, gc = gi % 14;
  const int aT = 16 * xg + (li >> 1), ahh = li & 1;
  const rsrc_t rWS = mkr(ws, WS_END);

  if (w >= 4) {
    unsigned slot = 0;
#define PIN(v) asm volatile("" : "+v"(v))
#define STEP(N) do { waitv<N>(); bar(); } while (0)
#define STEP2(c, N1, N0) do { if (c) waitv<N1>(); else waitv<N0>(); bar(); } while (0)
    auto nx = [&](unsigned s) { return (s + SLOT) & (4 * SLOT - 1); };
    const unsigned mAb = ldsbase + 4 * lw * 1024, mBb = ldsbase + BOFF + 4 * lw * 1024;
    int kc;
    auto iss1 = [&]() {
      dma3s(mAs + slot, sa[0], sa[1], sa[2], rX, sAo + kc * 128);
      dma2(mBs + slot, sb2[0], sb2[1], rW1, sBo + kc * (64 * D_ * 2));
      kc = (kc + 1 == 10) ? 0 : kc + 1; slot = nx(slot);
    };
    if (hasS) {
      if (lw < 3) { const unsigned go = lw * 1024 + lane * 16; dma(ldsbase + L_GW + lw * 1024, go < 1280 ? go : go - 1280, mkr(gw, D_ * 2), 0); }
      kc = sc >= 8 ? sc - 8 : sc + 2; slot = 2 * SLOT;
      iss1();
    }
    const unsigned tcode = lane < 4 ? (unsigned)((li * 4 + lw) * 4 + lane) * 128u : BIGOFF;
#define CODE_TOUCH(k) touch(ldsbase + L_DUM, tcode, rC, 0)
    unsigned ba[4], bq[4];
#pragma unroll
    for (int i = 0; i < 4; i++) { ba[i] = voffX(4 * lw + i, i); bq[i] = voffB(4 * lw + i, i, Q_ * 2); PIN(ba[i]); PIN(bq[i]); }
    const rsrc_t rWQ = mkr(wqkv, D_ * Q_ * 2);
    const unsigned qao = qr * 128 * (D_ * 2), qbo = qc * 256;
    int kq = qc % 10;
    auto issQ = [&]() {
      dma4s(mAb + slot, ba[0], ba[1], ba[2], ba[3], rX, qao + kq * 128);
      dma4(mBb + slot, bq[0], bq[1], bq[2], bq[3], rWQ, qbo + kq * (64 * Q_ * 2));
      kq = (kq + 1 == 10) ? 0 : kq + 1; slot = nx(slot);
    };
    const unsigned pid = (xg * 4 + li - 28) * 4 + lw, t50 = lane < 50 ? lane * 128 : BIGOFF, t45 = lane < 45 ? lane * 128 : BIGOFF;
    if (!hasS) {
      if (li >= 28) {
        for (int i = 0; i < 3; i++) touch(ldsbase + L_DUM, t50, rWQ, (i * 128 + pid) * 50 * 128);
        touch(ldsbase + L_DUM, t50, mkr(w2, D_ * D_ * 2), pid * 50 * 128);
      }
      slot = 2 * SLOT; issQ(); issQ(); issQ();
    }
    if (hasS) {
      STEP(5); iss1();
#pragma unroll 1
      for (int c = 1; c < 7; c++) { STEP(10); iss1(); }
      STEP(10); if (hasQ) issQ();
      STEP2(hasQ, 13, 5);
      STEP2(hasQ, 8, 0);
      if (hasQ) { issQ(); issQ(); } else {
        bar(); slot = 0;
      }
    }
    unsigned fa[3], fh[3], fb[4], sp2[2], bg[4], oa[2], ob[2], pv[6], l16 = lane * 16, kv0, kv1;
    auto voffP = [&](int pp, int i, unsigned ld) { const int ii = pp & 7;
      return (32 * (ii >> 2) + 16 * (l3 >> 2) + 4 * (ii & 3) + (l3 & 3)) * ld + bswz(pp, i); };
#pragma unroll
    for (int i = 0; i < 3; i++) { const int p = 3 * lw + i; fa[i] = l16 + ((p >> 1) * 20 + (p & 1) - i) * 1024; PIN(fa[i]); }
#pragma unroll
    for (int i = 0; i < 4; i++) { bg[i] = voffP(4 * lw + i, i, F_ * 2); PIN(bg[i]); }
#pragma unroll
    for (int i = 0; i < 2; i++) { sp2[i] = voffP(2 * lw + i, i, D_ * 2); PIN(sp2[i]); }
    {
      const int m = lane & 15, cur = (lane >> 4) & 1;
      kv1 = (cur || (aT >= 2 && m >= 8)) ? (unsigned)((cur ? aT : aT - 2) * (60 * 1024) + ((lane >> 5) * 16 + m) * 16) : BIGOFF;
      kv0 = kv1 + 512; PIN(kv0); PIN(kv1); PIN(l16);
      { const int cg = lane - 40; const bool pos = cg < 5 || (cg >= 10 && cg < 15);
        const int idx = lane < 40 ? 40 * sr + lane : (pos ? (2 * sr - 1) * 20 + cg : (2 * sr + 2) * 20 + cg);
        const bool val = lane < 40 || (lane < 60 && (pos ? sr > 0 : sr < 21));
        pv[0] = val ? WS_FH + idx * FS : BIGOFF; }
      { const int e = lane < 15 ? lane : lane - 10, part = e / 5, rr = lane < 15 ? (aT >> 2) : ((aT - 2) >> 2);
        const bool val = lane < 15 || (lane < 25 && aT >= 2);
        pv[1] = val ? WS_FQ + (rr * 30 + part * 10 + 5 * ahh + (e - part * 5)) * FS : BIGOFF; }
      PIN(pv[0]); PIN(pv[1]);
    }
    auto prep2 = [&]() {
      unsigned ln = lane; PIN(ln);
      const unsigned k3 = ln >> 3;
#pragma unroll
      for (int i = 0; i < 3; i++) { const int p = 3 * lw + i; fh[i] = ln * 16 + ((p >> 1) * 54 + (p & 1) - i) * 1024; PIN(fh[i]); }
#pragma unroll
      for (int i = 0; i < 4; i++) { const int p = 4 * lw + i; fb[i] = ln * 16 + ((p >> 1) * 20 + (p & 1) - i) * 1024; PIN(fb[i]); }
#pragma unroll
      for (int i = 0; i < 2; i++) { const int p = 2 * lw + i;
        oa[i] = (p * 20 + 4 * (p & 1) + k3) * (D_ * 2) + bswz(p, i);
        ob[i] = ((2 * p + (k3 >> 2)) * 20 + ((k3 >> 2) ? 0 : 16) + (k3 & 3)) * (D_ * 2) + bswz(p, i);
        PIN(oa[i]); PIN(ob[i]); }
      const unsigned r0 = (gr * 128) / 48, nf = ((gr * 128 + 127) / 48 - r0 + 1) * 20;
      pv[2] = ln < nf ? WS_FX + (r0 * 20 + ln) * FS : BIGOFF;
      pv[5] = ln < 16 ? (64 + ln < nf ? WS_FX + (r0 * 20 + 64 + ln) * FS : BIGOFF)
                      : (ln < (gc == 13 ? 20u : 24u) ? WS_FC + (gc * 8 + ln - 16) * FS : BIGOFF);
      pv[3] = ln < (sr == 21 ? 16u : 48u) ? WS_FO + (48 * sr + ln) * FS : BIGOFF;
      pv[4] = ln < (sr == 21 ? 27u : 54u) ? WS_FG + (((sr * 96) >> 6) * 27 + ln) * FS : BIGOFF;
#pragma unroll
      for (int j = 2; j < 6; j++) PIN(pv[j]);
    };
    unsigned elo = 0, ehi = 0;
    auto early_poll = [&](int j) { if (lw == 0) dma_sc1(ldsbase + L_POLL, pv[j], rWS, 0); };
    auto check = [&](int j) {
      const u32x2 f = *(volatile LAS u32x2*)(lds + L_POLL + lane * 16);
      return (bool)__all(pv[j] == BIGOFF || (f.x == (elo ^ pv[j]) && f.y == ehi));
    };
    unsigned a0, a1, x0, x1; int issued, checked;
    auto spin_init = [&](unsigned p0, unsigned p1) {
      a0 = p0; x0 = elo ^ p0; x1 = elo ^ p1; issued = 0; checked = 0;
      a1 = p1 == BIGOFF ? BIGOFF : p1 - 1024;
    };
    auto spin_run = [&](unsigned cap) {
      for (unsigned it = 0; it < cap; it++) {
        if (issued - checked < 7) { dma2s(ldsbase + L_SP + (issued & 7) * 2048, a0, a1, rWS, 0); issued++; }
        __builtin_amdgcn_s_sleep(PGAP);
        unsigned st;
        asm volatile("s_getreg_b32 %0, hwreg(HW_REG_IB_STS)" : "=s"(st) :: "memory");
        const int done = issued - (int)((((st & 15u) | ((st >> 18) & 48u)) + 1u) >> 1);
        if (done > checked) {
          checked = done;
          const char* pb = lds + L_SP + ((done - 1) & 7) * 2048 + lane * 16;
          const u32x2 f = *(volatile LAS u32x2*)pb, g = *(volatile LAS u32x2*)(pb + 1024);
          a0 = (f.x == x0 && f.y == ehi) ? BIGOFF : a0; a1 = (g.x == x1 && g.y == ehi) ? BIGOFF : a1;
          if (__all(a0 == BIGOFF && a1 == BIGOFF)) return true;
        }
      }
      return false;
    };
    auto spin = [&](int j) { spin_init(pv[j], BIGOFF); spin_run(SPIN_CAP); };
    auto proA = [&](rsrc_t rA, int ncg, int tile0, int ntile, int k, int nk, int nq) {
      unsigned sl = slot;
#pragma unroll 1
      for (int q = 0; q < nq; q++) {
        unsigned so = (tile0 * ncg + 2 * k) * 1024, m = ldsbase + sl;
#pragma unroll 1
        for (int t = 0; t < ntile; t++) { dma2s(m, l16, l16, rA, so); m += 2048; so += ncg * 1024; }
        sl = nx(sl); k = (k + 1 == nk) ? 0 : k + 1;
      }
    };
    auto rowc = [&]() {
      const int row = lw * 32 + (lane & 31);
      f32x2 t[10];
#pragma unroll
      for (int pi = 0; pi < 10; pi++) t[pi] = *(const f32x2*)(lds + L_ST + ((lane >> 5) * 10 + pi) * 1024 + row * 8);
      const char* cp = lds + L_CS + (lane >> 5) * 2048 + row * 4;
      const float c0 = *(const float*)cp, c1 = *(const float*)(cp + 1024);
      SB0;
      const f32x2 u = ((t[0] + t[1]) + (t[2] + t[3])) + ((t[4] + t[5]) + (t[6] + t[7])) + (t[8] + t[9]);
      const float cs = sum32(c0 + c1), s = sum32(u.x), q = sum32(u.y);
      const float mean = s * (1.0f / D_), var = fmaxf(q * (1.0f / D_) - mean * mean, 0.f) + 1e-5f, rstd = __builtin_amdgcn_rsqf(var);
      if (lane < 32) *(f32x4*)(lds + L_RC + row * 16) = (f32x4){mean, GK1 * rstd, GK2 * rstd * rstd * rstd, var * rstd};
      else *(float*)(lds + L_CF + row * 4) = cs;
    };
    auto jobS = [&](auto NPT, auto EARLYT, bool prev, int j, int nextj, int nk, int k0, rsrc_t rA, int ncg, unsigned (&va)[3], auto issB) {
      constexpr int NP = decltype(NPT)::value; constexpr bool EARLY = decltype(EARLYT)::value;
      const int k1 = (k0 + 1 == nk) ? 0 : k0 + 1, k2 = (k1 + 1 == nk) ? 0 : k1 + 1;
      const unsigned s1 = nx(slot), s2 = nx(s1), ao = 6 * sr * ncg * 1024;
      auto iss = [&]() {
        dma3s(mAs + slot, va[0], va[1], va[2], rA, ao + kc * 2048);
        issB(slot, kc);
        kc = (kc + 1 == nk) ? 0 : kc + 1; slot = nx(slot);
      };
      if (prev) STEP(2 * NP);
      int c0 = 0;
      if (EARLY && check(j)) {
        kc = k0;
        iss(); STEP(NP + 5); iss(); STEP(10); iss();
      } else {
        issB(slot, k0);
        if (j == 3 && prev) rowc();
        if (prev) { STEP(NP + 2); early_poll(j); }
        issB(s1, k1);
        if (j == 3) LG0;
        if (prev) STEP(2);
        issB(s2, k2);
        const bool ok = prev && check(j);
        if (ok) {
          dma3s(mAs + slot, va[0], va[1], va[2], rA, ao + k0 * 2048);
          dma3s(mAs + s1, va[0], va[1], va[2], rA, ao + k1 * 2048);
          dma3s(mAs + s2, va[0], va[1], va[2], rA, ao + k2 * 2048);
        } else if (lw == 0) { spin(j); proA(rA, ncg, 6 * sr, 6, k0, nk, 3); }
        else waitv<0>();
        slot = nx(s2); kc = (k2 + 1 == nk) ? 0 : k2 + 1;
        STEP2(ok, 6, 24); iss();
        STEP2(ok, 8, 17); iss();
        c0 = 2;
      }
#pragma unroll 1
      for (int c = c0; c < nk - 3; c++) { STEP(10); if (nextj >= 0 && c == nk - 5) early_poll(nextj); iss(); }
    };
    CODE_TOUCH(2);
    if (hasQ) {
#pragma unroll 1
      for (int c = 0; c < 7; c++) { STEP(16); issQ(); }
    }
    { const u32x2 ev = *(volatile LAS u32x2*)(lds + L_ONE); elo = __builtin_amdgcn_readfirstlane(ev.x); ehi = __builtin_amdgcn_readfirstlane(ev.y); }
    if (hasQ) {
      if (!hasS) {
        STEP(16); STEP(8); STEP(0); slot = 2 * SLOT;
        bar();
        if (li >= 28) {
          const rsrc_t r3 = mkr(w3, D_ * F_ * 2);
#pragma unroll 1
          for (int e = (li - 28) * 4 + lw; e < W3N * 27; e += 16) { const int st = e / 27, c = e - st * 27; touch(ldsbase + L_DUM, lane * (F_ * 2), r3, (((c >> 1) + st) % 10) * (64 * F_ * 2) + c * 128); }
        }
      }
    }
    if (hasS) {
      const rsrc_t rW2 = mkr(w2, D_ * D_ * 2);
      jobS(IC<8>{}, IC<0>{}, hasQ, 0, -1, 10, sc, mkr(ws + WS_H, S_ * D_ * 2), 20, fa,
           [&](unsigned sl, int k) { dma2(mBs + sl, sp2[0], sp2[1], rW2, sBo + k * (64 * D_ * 2)); });
    }
    const rsrc_t rW3 = mkr(w3, D_ * F_ * 2), rQ = mkr(ws + WS_QKV, S_ * Q_ * 2);
    const unsigned gbo = gc * 256;
    const int kg0 = gc % 10, kg1 = (kg0 + 1) % 10, kg2 = (kg0 + 2) % 10;
    {
      auto issQK = [&](int t0, int dt) {
#pragma unroll 1
        for (int t = t0; t < 15; t += dt) {
          if (t < 5) dma2s(ldsbase + 2 * SLOT + t * 2048, l16, l16, rQ, (aT * 60 + 10 * ahh) * 1024 + t * 2048);
          else dma2s(ldsbase + 2 * SLOT + t * 2048, kv0, kv1, rQ, (15 + 10 * ahh + t) * 1024 - 512);
        }
      };
      auto issV = [&](int t0, int dt) {
#pragma unroll 1
        for (int t = t0; t < 10; t += dt) dma2s(ldsbase + 3 * SLOT + t * 2048, kv0, kv1, rQ, (40 + 10 * ahh + t) * 1024 - 512);
      };
      if (hasS) { STEP(10); STEP(5); early_poll(1); STEP(0); }
      prep2();
      if (hasS && ADELAY) __builtin_amdgcn_s_sleep(ADELAY);
      if (hasS && check(1)) { issQK(lw, 4); issV(lw, 4); }
      else if (lw == 0) { spin(1); issQK(0, 1); issV(0, 1); }
      if (hasG) dma4(mBb, bg[0], bg[1], bg[2], bg[3], rW3, gbo + kg0 * (64 * F_ * 2));
      CODE_TOUCH(4);
      STEP2(hasG, 4, 0);
    }
    if (hasG) dma4(mBb + SLOT, bg[0], bg[1], bg[2], bg[3], rW3, gbo + kg1 * (64 * F_ * 2));
    const rsrc_t rX1 = mkr(ws + WS_X1, S_ * D_ * 2);
    bool got = false;
    slot = 0;
    spin_init(pv[2], pv[5]);
    if (hasG && lw == 0) got = spin_run(PRESPIN);
    bar();
    slot = 2 * SLOT;
    if (hasG) {
      const unsigned gao = 8 * gr * 20 * 1024;
      const int kg3 = (kg0 + 3) % 10;
      slot = 0;
      if (lw == 0) {
        if (!got) spin_run(SPIN_CAP);
        proA(rX1, 20, 8 * gr, 8, kg0, 10, 2);
      }
      dma4(mBb + 2 * SLOT, bg[0], bg[1], bg[2], bg[3], rW3, gbo + kg2 * (64 * F_ * 2));
      dma4(mBb + 3 * SLOT, bg[0], bg[1], bg[2], bg[3], rW3, gbo + kg3 * (64 * F_ * 2));
      int kg = (kg0 + 4) % 10;
      auto iss = [&]() {
        dma4s(mAb + slot, fb[0], fb[1], fb[2], fb[3], rX1, gao + kg * 2048);
        dma4(mBb + slot, bg[0], bg[1], bg[2], bg[3], rW3, gbo + kg * (64 * F_ * 2));
        kg = (kg + 1 == 10) ? 0 : kg + 1; slot = nx(slot);
      };
      STEP2(lw == 0, 24, 8);
      dma4s(mAb + 2 * SLOT, fb[0], fb[1], fb[2], fb[3], rX1, gao + kg2 * 2048);
      dma4s(mAb + 3 * SLOT, fb[0], fb[1], fb[2], fb[3], rX1, gao + kg3 * 2048);
      STEP(16); iss();
      STEP(12); iss();
      auto stat = [&]() {
#pragma unroll
        for (int i = 0; i < 5; i++) { const int pi = lw + 4 * i; dma_sc1(ldsbase + L_ST + pi * 1024, l16, rWS, WS_ST + pi * ST_LD + gr * 1024); }
        dma_sc1(ldsbase + L_CS + lw * 1024, lane < 32 ? l16 : BIGOFF, rWS, WS_CS + lw * 8192 + gc * 512);
      };
      {
#pragma unroll 1
        for (int c = 3; c < 5; c++) { STEP(16); iss(); }
        STEP(16); stat(); iss();
        STEP(22); iss();
        if (!hasS) { STEP(16); rowc(); STEP(8); LG0; STEP(0); }
      }
    }
    if (!hasG) {
      bar();
      const unsigned p2 = (xg * 4 + li) * 4 + lw;
      touch(ldsbase + L_DUM, t50, mkr(wo, D_ * D_ * 2), p2 * 50 * 128);
      for (int i = 0; i < 3; i++) touch(ldsbase + L_DUM, t45, mkr(w4, F_ * D_ * 2), (i * 128 + p2) * 45 * 128);
    }
    if (!hasG || li >= 28) {
      if (hasG) bar();
      const rsrc_t r4 = mkr(w4, F_ * D_ * 2);
      const int e1 = (hasG ? W4E : W4S) * 10;
#pragma unroll 1
      for (int e = (hasG ? W4S * 10 : 0) + (li & 3) * 4 + lw; e < e1; e += 16) {
        const int st = e / 10, c = e - st * 10;
        touch(ldsbase + L_DUM, lane * (D_ * 2), r4, ((c * 5 + st) % 27) * (64 * D_ * 2) + c * 128);
      }
    }
    if (hasS) {
      const rsrc_t rWO = mkr(wo, D_ * D_ * 2), rW4 = mkr(w4, F_ * D_ * 2);
      jobS(IC<8>{}, IC<0>{}, hasG, 3, 4, 10, sc, mkr(ws + WS_O, S_ * D_ * 2), 20, fa,
           [&](unsigned sl, int k) {
             const int hf = k >= 5, c = k - 5 * hf; const bool tl = c == 4;
             dma2(mBs + sl, tl ? ob[0] : oa[0], tl ? ob[1] : oa[1], rWO, sBo + (hf * 320 + (tl ? 0 : (c & 1) * 160 + (c >> 1) * 8)) * (D_ * 2)); });
      jobS(IC<5>{}, IC<1>{}, true, 4, -1, 27, (sc * 5) % 27, mkr(ws + WS_H2, S_ * F_ * 2), 54, fh,
           [&](unsigned sl, int k) { dma2(mBs + sl, sp2[0], sp2[1], rW4, sBo + k * (64 * D_ * 2)); });
      STEP(10); STEP(5); STEP(0);
    }
    waitv<0>();
    asm volatile(".Lend_ld:");
  } else {
    const int wr = w >> 1, wc = w & 1;
    const int m16 = lane & 15, G = lane >> 4;
    __builtin_amdgcn_s_setprio(3);
    const u64 E = loadE();
    if (w == 0 && lane == 0) *(LAS u64*)(lds + L_ONE) = E;
    if (lane < 40) *(LAS u32x4*)(lds + L_1S + w * 640 + lane * 16) = (u32x4){0x3F803F80u, 0x3F803F80u, 0x3F803F80u, 0x3F803F80u};
    asm volatile("s_waitcnt lgkmcnt(0)" ::: "memory");
    unsigned pend = 0, idle = 1;
    auto putflag = [&](unsigned foff) {
      if (lane == 0) __hip_atomic_store((GAS u64*)(ws + foff), E ^ foff, __ATOMIC_RELAXED, __HIP_MEMORY_SCOPE_AGENT);
    };
    auto flush = [&]() { if (!idle) { waitv<0>(); putflag(pend); idle = 1; } };
    auto tryflush = [&]() {
      unsigned st;
      asm volatile("s_getreg_b32 %0, hwreg(HW_REG_IB_STS)" : "=s"(st) :: "memory");
      if (__builtin_expect(((st & 0xC0000Fu) | idle) == 0, 0)) { waitv<0>(); putflag(pend); idle = 1; }
    };
#define PINV(v) asm volatile("" : "+v"(v))
#define LP(T, a) ((const LAS T*)(size_t)(a))
    const int q4 = (lane & 15) >> 2, p4 = lane & 3;
    const int bx = (((q4 >> 1) & 1) << 2) | ((G & 1) << 1);
    const unsigned sA0 = (G ^ ((m16 >> 1) & 7)) * 16, sA1 = ((4 + G) ^ ((m16 >> 1) & 7)) * 16;
    const unsigned brow = ldsbase + BOFF + (8 * G + q4) * 128 + 8 * (p4 & 1);
    const int cst = (G & 1) * 16 + (G >> 1) * 8;

    f32x4 acc[3][2];
    f32x4 aS[3], aC[2];
    f32x4 aQm[3];
    u32x2 xt[3][2];
    float gate[3];
    auto small = [&](auto NCHT, auto STATT, auto S0T, int rot, auto hook) {
      constexpr int NCH = decltype(NCHT)::value, S0 = decltype(S0T)::value; constexpr bool STAT = decltype(STATT)::value;
      unsigned pa0[2], pa1[2], pb0[2], pb1[2];
      pa0[0] = ldsbase + (STAT ? (wr * 48 + m16) * 128 + sA0 : wr * 6144 + lane * 16);
      pa1[0] = ldsbase + (STAT ? (wr * 48 + m16) * 128 + sA1 : wr * 6144 + lane * 16 + 1024);
      pb0[0] = brow + (((wc * 4 + 0 + (p4 >> 1)) ^ bx) * 16); pb1[0] = brow + (((wc * 4 + 2 + (p4 >> 1)) ^ bx) * 16);
      pa0[1] = pa0[0] + 2 * SLOT; pa1[1] = pa1[0] + 2 * SLOT; pb0[1] = pb0[0] + 2 * SLOT; pb1[1] = pb1[0] + 2 * SLOT;
      PINV(pa0[1]); if (STAT) PINV(pa1[1]); else { pa1[0] = pa0[0] + 1024; pa1[1] = pa0[1] + 1024; }
      PINV(pb0[1]); PINV(pb1[1]);
#pragma unroll
      for (int i = 0; i < 3; i++)
#pragma unroll
        for (int j = 0; j < 2; j++) acc[i][j] = (f32x4){0.f, 0.f, 0.f, 0.f};
      s16x8 ones = {0x3F80, 0x3F80, 0x3F80, 0x3F80, 0x3F80, 0x3F80, 0x3F80, 0x3F80};
      unsigned spp = ldsbase + ((m16 & 1) ? L_GW : L_1S) + G * 16 + rot * 128;
      if (STAT) {
#pragma unroll
        for (int i = 0; i < 3; i++) { aS[i] = (f32x4){0.f, 0.f, 0.f, 0.f}; aQm[i] = (f32x4){0.f, 0.f, 0.f, 0.f}; }
        aC[0] = aC[1] = (f32x4){0.f, 0.f, 0.f, 0.f};
        PINV(ones);
      }
      auto rd = [&](FragS& Fr, int c) {
        const int sl = (S0 + c) & 3, hi = sl >> 1; const unsigned so = (sl & 1) * SLOT;
#pragma unroll
        for (int ks = 0; ks < 2; ks++) {
          s16x4 l0 = __builtin_amdgcn_ds_read_tr16_b64_v4i16((LAS s16x4*)(size_t)(pb0[hi] + so + ks * 4096));
          s16x4 h0 = __builtin_amdgcn_ds_read_tr16_b64_v4i16((LAS s16x4*)(size_t)(pb0[hi] + so + ks * 4096 + 512));
          s16x4 l1 = __builtin_amdgcn_ds_read_tr16_b64_v4i16((LAS s16x4*)(size_t)(pb1[hi] + so + ks * 4096));
          s16x4 h1 = __builtin_amdgcn_ds_read_tr16_b64_v4i16((LAS s16x4*)(size_t)(pb1[hi] + so + ks * 4096 + 512));
          Fr.b[ks][0] = __builtin_shufflevector(l0, h0, 0, 1, 2, 3, 4, 5, 6, 7);
          Fr.b[ks][1] = __builtin_shufflevector(l1, h1, 0, 1, 2, 3, 4, 5, 6, 7);
#pragma unroll
          for (int i = 0; i < 3; i++) Fr.a[ks][i] = *LP(s16x8, (ks ? pa1[hi] : pa0[hi]) + so + i * 2048);
          if (STAT) Fr.sp[ks] = *LP(s16x8, spp + ks * 64);
        }
      };
      auto mm = [&](FragS& Fr) {
#pragma unroll
        for (int ks = 0; ks < 2; ks++) {
#pragma unroll
          for (int i = 0; i < 3; i++)
#pragma unroll
            for (int j = 0; j < 2; j++) acc[i][j] = __builtin_amdgcn_mfma_f32_16x16x32_bf16(Fr.b[ks][j], Fr.a[ks][i], acc[i][j], 0, 0, 0);
          if (STAT) {
#pragma unroll
            for (int i = 0; i < 3; i++) {
              aS[i] = __builtin_amdgcn_mfma_f32_16x16x32_bf16(Fr.sp[ks], Fr.a[ks][i], aS[i], 0, 0, 0);
              aQm[i] = __builtin_amdgcn_mfma_f32_16x16x32_bf16(Fr.a[ks][i], Fr.a[ks][i], aQm[i], 0, 0, 0);
            }
#pragma unroll
            for (int j = 0; j < 2; j++) aC[j] = __builtin_amdgcn_mfma_f32_16x16x32_bf16(Fr.b[ks][j], ones, aC[j], 0, 0, 0);
          }
        }
      };
      auto sched = [&]() {
#pragma unroll
        for (int i = 0; i < 7; i++) { SGB(0x8, 1, 0); SGB(0x100, 2, 0); }
        SGB(0x8, 5, 0);
      };
      auto stepS = [&](FragS& Fn, FragS& Fc, int c, int cs) {
        const int sl = (S0 + c) & 3, hi = sl >> 1; const unsigned so = (sl & 1) * SLOT;
        s16x4 bl[2][2], bh[2][2];
#pragma unroll
        for (int t = 0; t < 22; t++) {
          const int ks = t / 11, u = t % 11;
          if (u < 6) acc[u >> 1][u & 1] = __builtin_amdgcn_mfma_f32_16x16x32_bf16(Fc.b[ks][u & 1], Fc.a[ks][u >> 1], acc[u >> 1][u & 1], 0, 0, 0);
          else if (u < 9) aS[u - 6] = __builtin_amdgcn_mfma_f32_16x16x32_bf16(Fc.sp[ks], Fc.a[ks][u - 6], aS[u - 6], 0, 0, 0);
          else aC[u - 9] = __builtin_amdgcn_mfma_f32_16x16x32_bf16(Fc.b[ks][u - 9], ones, aC[u - 9], 0, 0, 0);
          if (t < 8) {
            const int k2 = t >> 2, r = t & 3;
            if (r < 2) {
              bl[k2][r] = __builtin_amdgcn_ds_read_tr16_b64_v4i16((LAS s16x4*)(size_t)((r ? pb1[hi] : pb0[hi]) + so + k2 * 4096));
              bh[k2][r] = __builtin_amdgcn_ds_read_tr16_b64_v4i16((LAS s16x4*)(size_t)((r ? pb1[hi] : pb0[hi]) + so + k2 * 4096 + 512));
              Fn.b[k2][r] = __builtin_shufflevector(bl[k2][r], bh[k2][r], 0, 1, 2, 3, 4, 5, 6, 7);
            } else if (r == 2) {
              Fn.a[k2][0] = *LP(s16x8, (k2 ? pa1[hi] : pa0[hi]) + so); Fn.a[k2][1] = *LP(s16x8, (k2 ? pa1[hi] : pa0[hi]) + so + 2048);
            } else {
              Fn.a[k2][2] = *LP(s16x8, (k2 ? pa1[hi] : pa0[hi]) + so + 4096); Fn.sp[k2] = *LP(s16x8, spp + cs * 128 + k2 * 64);
            }
          } else if (t < 14) { const int e = t - 8; aQm[e % 3] = __builtin_amdgcn_mfma_f32_16x16x32_bf16(Fc.a[e / 3][e % 3], Fc.a[e / 3][e % 3], aQm[e % 3], 0, 0, 0); }
          SB0;
        }
      };
      FragS F0, F1;
      bar(); SB0;
      if (STAT) {
#pragma unroll
        for (int i = 0; i < 3; i++)
#pragma unroll
          for (int j = 0; j < 2; j++)
            xt[i][j] = *LP(u32x2, ldsbase + S0 * SLOT + (wr * 48 + 16 * i + m16) * 128 + (((wc * 4 + 2 * j + (G >> 1)) ^ ((m16 >> 1) & 7)) * 16) + (G & 1) * 8);
      }
      rd(F0, 0); SB0;
      if (STAT) waitv<0>();
      constexpr int NQ = (NCH - 1) / 4;
      auto quad = [&](int c) {
        if (STAT) {
          LG0; bar(); SB0; stepS(F1, F0, 1, 1);
          LG0; bar(); SB0; stepS(F0, F1, 2, 2);
          LG0; bar(); SB0; stepS(F1, F0, 3, 3);
          LG0; bar(); SB0; stepS(F0, F1, 0, 4);
          spp += 512;
          return;
        }
        LG0; bar(); SB0; rd(F1, 1); mm(F0); sched(); SB0; hook(c);
        LG0; bar(); SB0; rd(F0, 2); mm(F1); sched(); SB0; hook(c + 1);
        LG0; bar(); SB0; rd(F1, 3); mm(F0); sched(); SB0; hook(c + 2);
        LG0; bar(); SB0; rd(F0, 0); mm(F1); sched(); SB0; hook(c + 3);
      };
      if (NCH == 10 && !STAT) { quad(1); quad(5); }
      else {
#pragma unroll 1
        for (int q = 0; q < NQ; q++) quad(0);
      }
      LG0; bar(); SB0;
      if (STAT) stepS(F1, F0, 1, 1); else { rd(F1, 1); mm(F0); sched(); }
      SB0;
      if (NCH == 27) { LG0; bar(); SB0; rd(F0, 2); mm(F1); sched(); SB0; mm(F0); }
      else mm(F1);
      SB0;
    };
    f32x4 bacc[4][4];
    auto big = [&](auto FNT, auto S0T, auto late) {
      constexpr bool FN = decltype(FNT)::value; constexpr int S0 = decltype(S0T)::value;
#pragma unroll
      for (int i = 0; i < 4; i++)
#pragma unroll
        for (int j = 0; j < 4; j++) bacc[i][j] = (f32x4){0.f, 0.f, 0.f, 0.f};
      unsigned a0[2], a1[2], bo[2][4];
      a0[0] = ldsbase + (FN ? wr * 8192 + lane * 16 : (wr * 64 + m16) * 128 + sA0);
      a1[0] = ldsbase + (FN ? wr * 8192 + lane * 16 + 1024 : (wr * 64 + m16) * 128 + sA1);
      a0[1] = a0[0] + 2 * SLOT; a1[1] = a1[0] + 2 * SLOT;
      PINV(a0[1]); if (!FN) PINV(a1[1]); else { a1[0] = a0[0] + 1024; a1[1] = a0[1] + 1024; }
#pragma unroll
      for (int j = 0; j < 4; j++) { bo[0][j] = brow + wc * 8192 + (((2 * j + (p4 >> 1)) ^ bx) * 16); bo[1][j] = bo[0][j] + 2 * SLOT; PINV(bo[1][j]); }
      auto rd = [&](FragB& Fr, int c, int ks) {
        const int sl = (S0 + c) & 3, hi = sl >> 1; const unsigned so = (sl & 1) * SLOT;
#pragma unroll
        for (int j = 0; j < 4; j++) {
          s16x4 lo = __builtin_amdgcn_ds_read_tr16_b64_v4i16((LAS s16x4*)(size_t)(bo[hi][j] + so + ks * 4096));
          s16x4 hh = __builtin_amdgcn_ds_read_tr16_b64_v4i16((LAS s16x4*)(size_t)(bo[hi][j] + so + ks * 4096 + 512));
          Fr.b[j] = __builtin_shufflevector(lo, hh, 0, 1, 2, 3, 4, 5, 6, 7);
        }
#pragma unroll
        for (int i = 0; i < 4; i++) Fr.a[i] = *LP(s16x8, (ks ? a1[hi] : a0[hi]) + so + i * 2048);
      };
      auto mm = [&](FragB& Fr) {
#pragma unroll
        for (int i = 0; i < 4; i++)
#pragma unroll
          for (int j = 0; j < 4; j++) bacc[i][j] = __builtin_amdgcn_mfma_f32_16x16x32_bf16(Fr.b[j], Fr.a[i], bacc[i][j], 0, 0, 0);
      };
      auto sched = [&]() {
#pragma unroll
        for (int i = 0; i < 12; i++) { SGB(0x8, 1, 0); SGB(0x100, 1, 0); }
        SGB(0x8, 4, 0);
      };
      FragB F0, F1;
      auto one = [&](int c) {
        rd(F1, c, 1); mm(F0); sched(); SB0;
        LG0; bar(); SB0;
        rd(F0, c + 1, 0); mm(F1); sched(); SB0;
      };
      bar(); SB0;
      rd(F0, 0, 0); SB0;
#pragma unroll 1
      for (int q = 0; q < 2; q++) { one(0); one(1); tryflush(); one(2); one(3); tryflush(); }
      one(0);
      rd(F1, 1, 1); mm(F0); sched(); SB0;
      late(); SB0;
      mm(F1); SB0;
    };
    auto nohook = [&](int) {};
    const int scg = 2 * sc + wc;
    const int svt = 6 * sr + 3 * wr;

    auto colsum = [&]() {
      const int cu = 4 * xg + (li - 28);
      if (li >= 28 && cu < 27) {
        float cs[8];
#pragma unroll
        for (int e = 0; e < 8; e++) cs[e] = 0.f;
        const GAS char* src = (const GAS char*)w3 + (size_t)(w * 160 + l3) * (F_ * 2) + cu * 128 + (lane & 7) * 16;
        u32x4 v[20];
#pragma unroll
        for (int i = 0; i < 20; i++) v[i] = *(const GAS u32x4*)(src + (size_t)i * 8 * (F_ * 2));
#pragma unroll
        for (int i = 0; i < 20; i++) {
#pragma unroll
          for (int e = 0; e < 4; e++) { cs[2 * e] += bflo(v[i][e]); cs[2 * e + 1] += bfhi(v[i][e]); }
        }
#pragma unroll
        for (int e = 0; e < 8; e++) { cs[e] += __shfl_xor(cs[e], 8); cs[e] = sum32(sum16(cs[e])); }
        if (lane < 16) {
          const bool hi = lane >= 8;
          const u32x4 sv = {__float_as_uint(hi ? cs[4] : cs[0]), __float_as_uint(hi ? cs[5] : cs[1]), __float_as_uint(hi ? cs[6] : cs[2]), __float_as_uint(hi ? cs[7] : cs[3])};
          __builtin_amdgcn_raw_buffer_store_b128(sv, rWS, WS_CS + w * 8192 + cu * 256 + (lane & 7) * 32 + (lane >> 3) * 16, 0, 16);
        }
        pend = WS_FC + (cu * 4 + w) * FS; idle = 0;
      }
    };
    if (hasS) {
      const int sh = scg < 5 ? 1 : (scg < 10 ? -1 : (scg < 15 ? 2 : -2));
      unsigned hoff[3];
#pragma unroll
      for (int i = 0; i < 3; i++) {
        const int r0 = vrow((svt + i) * 16 + m16), r1 = r0 + sh, v1 = rowv(r1);
        hoff[i] = (r0 < S_ && r1 >= 0) ? (unsigned)(((v1 >> 4) * 20 + scg) * 1024 + ((v1 & 15) + 16 * G) * 16) : BIGOFF;
        PINV(hoff[i]);
      }
      small(IC<10>{}, IC<1>{}, IC<0>{}, sc, nohook);
      const rsrc_t rH = mkr(ws + WS_H, S_ * D_ * 2);
      float mean[3], ga[3], gb[3], gs[3];
#pragma unroll
      for (int i = 0; i < 3; i++) {
        mean[i] = aS[i][0] * (1.0f / D_);
        const int r = m16 & 3;
        const float qd = r == 0 ? aQm[i][0] : (r == 1 ? aQm[i][1] : (r == 2 ? aQm[i][2] : aQm[i][3]));
        const float q = sum32(sum16(G == (m16 >> 2) ? qd : 0.f));
        const float var = fmaxf(q * (1.0f / D_) - mean[i] * mean[i], 0.f) + 1e-5f, rstd = __builtin_amdgcn_rsqf(var);
        gs[i] = var * rstd; ga[i] = GK1 * rstd; gb[i] = GK2 * rstd * rstd * rstd;
      }
      SB0;
#pragma unroll
      for (int i = 0; i < 3; i++) {
        const f32x4 z0 = acc[i][0] - aC[0] * mean[i], z1 = acc[i][1] - aC[1] * mean[i];
        __builtin_amdgcn_raw_buffer_store_b128(gelu8(z0, z1, ga[i], gb[i], gs[i]), rH, hoff[i], 0, 16);
      }
#pragma unroll
      for (int i = 0; i < 3; i++) gate[i] = __builtin_amdgcn_rcpf(1.0f + __builtin_amdgcn_exp2f(-1.4426950408889634f * aS[i][1]));
      if (wr == 0 && sr == (sh > 0 ? 0 : 21)) {
        const int v1 = rowv(sh > 0 ? m16 : S_ - 1 - m16);
        const unsigned off = m16 < abs(sh) ? (unsigned)(((v1 >> 4) * 20 + scg) * 1024 + ((v1 & 15) + 16 * G) * 16) : BIGOFF;
        __builtin_amdgcn_raw_buffer_store_b128((u32x4){0u, 0u, 0u, 0u}, rH, off, 0, 16);
      }
      pend = WS_FH + ((2 * sr + wr) * 20 + scg) * FS; idle = 0;
      if (!hasQ) { flush(); waitv<0>(); bar(); }
    } else colsum();
    u32x4 qpk[8];
    const rsrc_t rQ = mkr(ws + WS_QKV, S_ * Q_ * 2);
    const unsigned qoff = ((8 * qr + 4 * wr) * 60 + 4 * qc + 2 * wc) * 1024 + (cst * 2 + m16) * 16;
    const unsigned qflag = WS_FQ + ((2 * qr + wr) * 30 + 2 * qc + wc) * FS;
    auto qstore = [&](int k) { __builtin_amdgcn_raw_buffer_store_b128(qpk[k], rQ, qoff + (k >> 1) * (60 * 1024) + (k & 1) * 1024, 0, 16); };
    if (hasQ) {
      big(IC<0>{}, IC<2>{}, [&]() {});
      flush();
#pragma unroll
      for (int i = 0; i < 4; i++)
#pragma unroll
        for (int jp = 0; jp < 2; jp++) {
          const f32x4 u = bacc[i][2 * jp], v = bacc[i][2 * jp + 1];
          qpk[2 * i + jp] = pack2(cvt_pk(u[0], u[1]), cvt_pk(u[2], u[3]), cvt_pk(v[0], v[1]), cvt_pk(v[2], v[3]));
        }
#pragma unroll
      for (int k = 0; k < 8; k++) qstore(k);
      pend = qflag; idle = 0;
      if (!hasS) { flush(); waitv<0>(); bar(); }
    }
    if (hasS) {
      small(IC<10>{}, IC<0>{}, IC<0>{}, 0, [&](int c) { if (c >= 2) tryflush(); });
      flush();
      const rsrc_t rX1 = mkr(ws + WS_X1, S_ * D_ * 2);
#pragma unroll
      for (int i = 0; i < 3; i++) {
#pragma unroll
        for (int j = 0; j < 2; j++)
          xt[i][j] = (u32x2){cvt_pk(bflo(xt[i][j].x) + acc[i][j][0], bfhi(xt[i][j].x) + acc[i][j][1]),
                             cvt_pk(bflo(xt[i][j].y) + acc[i][j][2], bfhi(xt[i][j].y) + acc[i][j][3])};
        __builtin_amdgcn_raw_buffer_store_b128((u32x4){xt[i][0].x, xt[i][0].y, xt[i][1].x, xt[i][1].y}, rX1, ((svt + i) * 20 + scg) * 1024 + lane * 16, 0, 16);
      }
      SB0;
      float s[3], q[3];
#pragma unroll
      for (int i = 0; i < 3; i++) { s[i] = dot2(xt[i][0].x, 0x3F803F80u, 0.f); q[i] = dot2(xt[i][0].x, xt[i][0].x, 0.f); }
#pragma unroll
      for (int i = 0; i < 3; i++) { s[i] = dot2(xt[i][0].y, 0x3F803F80u, s[i]); q[i] = dot2(xt[i][0].y, xt[i][0].y, q[i]); }
#pragma unroll
      for (int i = 0; i < 3; i++) { s[i] = dot2(xt[i][1].x, 0x3F803F80u, s[i]); q[i] = dot2(xt[i][1].x, xt[i][1].x, q[i]); }
#pragma unroll
      for (int i = 0; i < 3; i++) { s[i] = dot2(xt[i][1].y, 0x3F803F80u, s[i]); q[i] = dot2(xt[i][1].y, xt[i][1].y, q[i]); }
#pragma unroll
      for (int i = 0; i < 3; i++) { s[i] = sum16(s[i]); q[i] = sum16(q[i]); }
#pragma unroll
      for (int i = 0; i < 3; i++) { s[i] = sum32(s[i]); q[i] = sum32(q[i]); }
      {
        const u32x2 sv = {__float_as_uint(G == 0 ? s[0] : (G == 1 ? s[1] : s[2])), __float_as_uint(G == 0 ? q[0] : (G == 1 ? q[1] : q[2]))};
        __builtin_amdgcn_raw_buffer_store_b64(sv, rWS, G < 3 ? WS_ST + scg * ST_LD + (svt * 16 + lane) * 8 : BIGOFF, 0, 16);
      }
      pend = WS_FX + ((2 * sr + wr) * 20 + scg) * FS; idle = 0;
      flush();
    }
    {
      const int hl = 4 * w + G, pf = (5 * hl + 1) >> 1;
      const int tl = (hl & 1) ? -1 : 2, th = (hl & 1) * 8;
      const char* qp = lds + 2 * SLOT + pf * 256 + m16 * 16;
      const char* kp = lds + 2 * SLOT + 10240 + pf * 512 + (9 + m16) * 16;
      const char* kt = kp + tl * 512 + th;
      const char* vp = lds + 3 * SLOT + pf * 512 + (9 + m16) * 16;
      const char* vt = vp + tl * 512 + th;
      const int srow_g = vrow(aT * 16 + m16);
      bar(); SB0;
      const u32x4 q0 = *(const u32x4*)qp, q1 = *(const u32x4*)(qp + 256);
      const u32x2 q2 = *(const u32x2*)(qp + tl * 256 + th);
      u32x4 k0[8], k1[8]; u32x2 k2[8];
#pragma unroll
      for (int ww = 0; ww < 8; ww++) { k0[ww] = *(const u32x4*)(kp + (7 - ww) * 16); k1[ww] = *(const u32x4*)(kp + (7 - ww) * 16 + 512); k2[ww] = *(const u32x2*)(kt + (7 - ww) * 16); }
      SB0;
      float la[8], lb[8], lg[8];
#pragma unroll
      for (int h = 0; h < 2; h++) {
#pragma unroll
        for (int ww = 4 * h; ww < 4 * h + 4; ww++) { la[ww] = dot2(q2.x, k2[ww].x, 0.f); lb[ww] = dot2(q2.y, k2[ww].y, 0.f); }
#pragma unroll
        for (int d = 0; d < 4; d++)
#pragma unroll
          for (int ww = 4 * h; ww < 4 * h + 4; ww++) { la[ww] = dot2(q0[d], k0[ww][d], la[ww]); lb[ww] = dot2(q1[d], k1[ww][d], lb[ww]); }
        SB0;
      }
      u32x4 v0[8], v1[8]; u32x2 v2[8];
#pragma unroll
      for (int ww = 0; ww < 8; ww++) { v0[ww] = *(const u32x4*)(vp + (7 - ww) * 16); v1[ww] = *(const u32x4*)(vp + (7 - ww) * 16 + 512); v2[ww] = *(const u32x2*)(vt + (7 - ww) * 16); }
      LG0; bar(); SB0;
#pragma unroll
      for (int ww = 0; ww < 8; ww++) lg[ww] = (srow_g - 2 * ww >= 0) ? (la[ww] + lb[ww]) * (0.22360679774997896f * 1.4426950408889634f) : -1e30f;
      const float mx = fmaxf(fmaxf(fmaxf(lg[0], lg[1]), fmaxf(lg[2], lg[3])), fmaxf(fmaxf(lg[4], lg[5]), fmaxf(lg[6], lg[7])));
#pragma unroll
      for (int ww = 0; ww < 8; ww++) lg[ww] = __builtin_amdgcn_exp2f(lg[ww] - mx);
      const float inv = __builtin_amdgcn_rcpf(((lg[0] + lg[1]) + (lg[2] + lg[3])) + ((lg[4] + lg[5]) + (lg[6] + lg[7])));
      unsigned pp[4];
#pragma unroll
      for (int k = 0; k < 4; k++) pp[k] = cvt_pk(lg[2 * k] * inv, lg[2 * k + 1] * inv);
      SB0; SB0;
      float o[20];
#pragma unroll
      for (int k = 0; k < 4; k++) {
        unsigned pm[20];
#pragma unroll
        for (int d = 0; d < 10; d++) {
          const unsigned ta = d < 4 ? v0[2 * k][d] : (d < 8 ? v1[2 * k][d - 4] : v2[2 * k][d - 8]);
          const unsigned tb = d < 4 ? v0[2 * k + 1][d] : (d < 8 ? v1[2 * k + 1][d - 4] : v2[2 * k + 1][d - 8]);
          pm[2 * d] = __builtin_amdgcn_perm(tb, ta, 0x05040100u); pm[2 * d + 1] = __builtin_amdgcn_perm(tb, ta, 0x07060302u);
        }
        SB0;
#pragma unroll
        for (int d = 0; d < 20; d++) o[d] = dot2(pm[d], pp[k], k ? o[d] : 0.f);
        SB0;
      }
      const rsrc_t rO = mkr(ws + WS_O, S_ * D_ * 2);
      const unsigned ooff = (aT * 20 + 10 * ahh) * 1024;
      __builtin_amdgcn_raw_buffer_store_b128((u32x4){cvt_pk(o[0], o[1]), cvt_pk(o[2], o[3]), cvt_pk(o[4], o[5]), cvt_pk(o[6], o[7])}, rO, ooff + w * 1024 + lane * 16, 0, 16);
      __builtin_amdgcn_raw_buffer_store_b128((u32x4){cvt_pk(o[8], o[9]), cvt_pk(o[10], o[11]), cvt_pk(o[12], o[13]), cvt_pk(o[14], o[15])}, rO, ooff + (4 + w) * 1024 + lane * 16, 0, 16);
      {
        const unsigned t0 = cvt_pk(o[16], o[17]), t1 = cvt_pk(o[18], o[19]);
        auto r0 = __builtin_amdgcn_permlane16_swap(t0, t0, false, false);
        auto r1 = __builtin_amdgcn_permlane16_swap(t1, t1, false, false);
        __builtin_amdgcn_raw_buffer_store_b128((u32x4){r0[0], r1[0], r0[1], r1[1]}, rO,
                                               (G & 1) ? BIGOFF : ooff + 8 * 1024 + w * 512 + (G >> 1) * 256 + m16 * 16, 0, 16);
      }
      pend = WS_FO + ((aT * 2 + ahh) * 4 + w) * FS; idle = 0;
      if (!hasG) { flush(); waitv<0>(); bar(); }
    }
    if (hasG) {
      f32x4 rc[4], cs[4];
      big(IC<1>{}, IC<0>{}, [&]() {
#pragma unroll
        for (int i = 0; i < 4; i++) {
          rc[i] = *(const f32x4*)(lds + L_RC + (wr * 64 + 16 * i + m16) * 16);
          cs[i] = *(const f32x4*)(lds + L_CF + (wc * 64 + 16 * i + 4 * G) * 4);
        }
      });
      flush();
      if (gc * 128 + wc * 64 < F_) {
        const rsrc_t rG = mkr(ws + WS_H2, S_ * F_ * 2);
        const unsigned goff = ((8 * gr + 4 * wr) * 54 + 4 * gc + 2 * wc) * 1024 + lane * 16;
#pragma unroll
        for (int i = 0; i < 4; i++)
#pragma unroll
          for (int jp = 0; jp < 2; jp++) {
            const f32x4 z0 = bacc[i][2 * jp] - cs[2 * jp] * rc[i].x, z1 = bacc[i][2 * jp + 1] - cs[2 * jp + 1] * rc[i].x;
            const u32x4 pk = gelu8(z0, z1, rc[i].y, rc[i].z, rc[i].w);
            __builtin_amdgcn_raw_buffer_store_b128(pk, rG, goff + i * (54 * 1024) + jp * 1024, 0, 16);
          }
        pend = WS_FG + ((2 * gr + wr) * 27 + 2 * gc + wc) * FS; idle = 0;
      }
    }
    if (hasS) {
      small(IC<10>{}, IC<0>{}, IC<2>{}, 0, [&](int c) { if (c <= 6) tryflush(); });
      flush();
      f32x4 tacc[3][2];
#pragma unroll
      for (int i = 0; i < 3; i++)
#pragma unroll
        for (int j = 0; j < 2; j++) {
          const float g = gate[i], h = 1.0f - g;
          tacc[i][j] = (f32x4){g * acc[i][j][0] + h * bflo(xt[i][j].x), g * acc[i][j][1] + h * bfhi(xt[i][j].x),
                               g * acc[i][j][2] + h * bflo(xt[i][j].y), g * acc[i][j][3] + h * bfhi(xt[i][j].y)};
        }
#pragma unroll
      for (int i = 0; i < 3; i++) { PINV(tacc[i][0]); PINV(tacc[i][1]); }
      small(IC<27>{}, IC<0>{}, IC<0>{}, 0, nohook);
      const rsrc_t rOut = mkr(out, S_ * D_ * 2);
#pragma unroll
      for (int i = 0; i < 3; i++) {
        const float h = 1.0f - gate[i];
        unsigned pk[2][2];
#pragma unroll
        for (int j = 0; j < 2; j++) {
          pk[j][0] = cvt_pk(tacc[i][j][0] + h * acc[i][j][0], tacc[i][j][1] + h * acc[i][j][1]);
          pk[j][1] = cvt_pk(tacc[i][j][2] + h * acc[i][j][2], tacc[i][j][3] + h * acc[i][j][3]);
        }
        __builtin_amdgcn_raw_buffer_store_b128(pack2(pk[0][0], pk[0][1], pk[1][0], pk[1][1]), rOut,
                                               vrow((svt + i) * 16 + m16) * (D_ * 2) + (scg * 32 + cst) * 2, 0, 0);
      }
    }
    flush();
    if (li >= 28) { waitv<0>(); bar(); }
    if (b == 255 && w == 0) {
      const unsigned elo = (unsigned)E, ehi = (unsigned)(E >> 32);
      for (unsigned spin = 0; spin < SPIN_CAP; spin++) {
        bool ok = true;
#pragma unroll
        for (int t = 0; t < 16; t++) {
          const u32x2 f = __builtin_amdgcn_raw_buffer_load_b64(rWS, WS_FO + (t * 64 + lane) * FS, 0, 16);
          ok = ok && f.x == (elo ^ (WS_FO + (t * 64 + lane) * FS)) && f.y == ehi;
        }
        if (__all(ok)) break;
        __builtin_amdgcn_s_sleep(4);
      }
      if (lane == 0) __hip_atomic_store((GAS u64*)(ws + WS_EPOCH), E, __ATOMIC_RELAXED, __HIP_MEMORY_SCOPE_AGENT);
    }
    asm volatile(".Lend_cp:");
  }
}

extern "C" void kernel_launch(void* const* d_in, const int* in_sizes, int n_in, void* d_out, int out_size, void* d_ws, size_t ws_size,
                              hipStream_t stream) {
  (void)in_sizes; (void)n_in; (void)out_size; (void)ws_size;
  (void)hipFuncSetAttribute((const void*)fused_kernel, hipFuncAttributeMaxDynamicSharedMemorySize, LDS_BYTES);
  fused_kernel<<<256, 512, LDS_BYTES, stream>>>((const u16*)d_in[0], (const u16*)d_in[1], (const u16*)d_in[2], (const u16*)d_in[4],
                                                (const u16*)d_in[5], (const u16*)d_in[6], (const u16*)d_in[7], (const u16*)d_in[8],
                                                (u16*)d_out, (char*)d_ws);
}
```
